```python
import torch

def _softmax(x, axis=-1):
    return torch.softmax(x, dim=axis)
def _gelu(x):
    return torch.nn.functional.gelu(x, approximate="tanh")
def _silu(x):
    return torch.nn.functional.silu(x)
def _sigmoid(x):
    return torch.sigmoid(x)
def _layernorm(x, axis=-1, eps=1e-5):
    m = x.mean(dim=axis, keepdim=True)
    v = x.var(dim=axis, keepdim=True, unbiased=False)
    return (x - m) / torch.sqrt(v + eps)
def _scatter_add(tgt, idx, src, axis=0):
    return tgt.clone().index_add_(axis, idx, src)
def _segment_sum(src, seg, n):
    out = torch.zeros((n,) + src.shape[1:], dtype=src.dtype)
    return out.index_add_(0, seg, src)
def _pad(x, pw):
    flat = tuple(v for pair in reversed(pw) for v in pair)
    return torch.nn.functional.pad(x, flat)

kernel_name = 'parallel_sum__ssm_conv_h3__mixer_channel_mlp__prompt'

def setup_inputs():

    _w = (lambda a: (lambda t: t.to(torch.bfloat16) if t.is_floating_point() else t)(torch.as_tensor(a)))
    def _A_setup():
        B, S, D = 1, 2048, 512
        ks = 4
        return {
            "q": _w((lambda *s: torch.randn(*s, dtype=torch.bfloat16) * 0.05)(B, S, D)),
            "k": _w((lambda *s: torch.randn(*s, dtype=torch.bfloat16) * 0.2)(B, S, D)),
            "v": _w((lambda *s: torch.randn(*s, dtype=torch.bfloat16) * 0.2)(B, S, D)),
            "shift_k": _w((lambda *s: torch.randn(*s, dtype=torch.bfloat16) * 0.2)(D, ks) * 0.5),
            "ssm_k": _w((lambda *s: torch.randn(*s, dtype=torch.bfloat16) * 0.8)(D, S) * (1.0 / S ** 0.5)),
            "ssm_D": _w((lambda *s: torch.randn(*s, dtype=torch.bfloat16) * 0.2)(D)),
        }
    _A_in = _A_setup()

    def _B_setup():
        B, S, D, F = 1, 2048, 512, 1792
        return {
            "x": _w((lambda *s: torch.randn(*s, dtype=torch.bfloat16) * 0.05)(B, S, D)),
            "w1": _w((lambda *s: torch.randn(*s, dtype=torch.bfloat16) * 0.8)(D, F) * (D ** -0.5)),
            "b1": _w((lambda *s: torch.randn(*s, dtype=torch.bfloat16) * 0.4)(F)),
            "w2": _w((lambda *s: torch.randn(*s, dtype=torch.bfloat16) * 0.05)(F, D) * (F ** -0.5)),
            "b2": _w((lambda *s: torch.randn(*s, dtype=torch.bfloat16) * 0.05)(D)),
        }
    _B_in = _B_setup()

    return {
        "A_q": _A_in["q"],
        "A_k": _A_in["k"],
        "A_v": _A_in["v"],
        "A_shift_k": _A_in["shift_k"],
        "A_ssm_k": _A_in["ssm_k"],
        "A_ssm_D": _A_in["ssm_D"],
        "B_x": _B_in["x"],
        "B_w1": _B_in["w1"],
        "B_b1": _B_in["b1"],
        "B_w2": _B_in["w2"],
        "B_b2": _B_in["b2"],
    }

def reference(A_q, A_k, A_v, A_shift_k, A_ssm_k, A_ssm_D, B_x, B_w1, B_b1, B_w2, B_b2):
    def _A_ref(q, k, v, shift_k, ssm_k, ssm_D):
        B, S, D = q.shape
        ks = shift_k.shape[-1]
        k_pad = _pad(k, ((0, 0), (ks - 1, 0), (0, 0)))
        k_shift = torch.zeros_like(k)
        for t in range(ks):
            k_shift = k_shift + k_pad[:, t:t + S, :] * shift_k[:, ks - 1 - t][None, None, :]
        kv = k_shift * v
        L = 2 * S
        kv_pad = _pad(kv, ((0, 0), (0, S), (0, 0)))
        kf_pad = _pad(ssm_k, ((0, 0), (0, S)))
        kv_f = (lambda x, axes=(-2,-1): torch.fft.rfft2(x.float(), dim=axes))(kv_pad[:, None, :, :], axes=(1, 2))
        kf_f = (lambda x, axes=(-2,-1): torch.fft.rfft2(x.float(), dim=axes))(kf_pad[:, None, :], axes=(1, 2))
        kf_f = (lambda x, *d: x.permute(*d))(kf_f, 1, 2, 0)
        y_f = kv_f * kf_f[None, :, :, :]
        y = (lambda x, s, axes=(-2,-1): torch.fft.irfft2(x, s=s, dim=axes).to(torch.bfloat16))(y_f, (1, L), axes=(1, 2))[:, 0, :S, :]
        y = y + kv * ssm_D[None, None, :]
        return q * y

    def _B_ref(x, w1, b1, w2, b2):
        y = _layernorm(x, axis=-1)
        h = _gelu(torch.einsum("bsd,df->bsf", y, w1) + b1)
        return torch.einsum("bsf,fd->bsd", h, w2) + b2

    x = A_q
    xn = _layernorm(x, axis=-1)
    return x + _A_ref(xn, A_k, A_v, A_shift_k, A_ssm_k, A_ssm_D) + _B_ref(xn, B_w1, B_b1, B_w2, B_b2)
```

```cpp
#include <hip/hip_runtime.h>
#include <hip/hip_bf16.h>
typedef unsigned short u16;
constexpr int S=2048, D=512, F=1792;
__device__ __forceinline__ float bf2f(u16 b){ return __builtin_bit_cast(float,(unsigned)b<<16); }
__device__ __forceinline__ u16 f2bf(float f){ unsigned u=__builtin_bit_cast(unsigned,f); u+=0x7FFFu+((u>>16)&1u); return (u16)(u>>16); }
__device__ __forceinline__ float rb(float f){ return bf2f(f2bf(f)); }

__global__ void k_ln(const u16* q, const u16* k, const u16* v, const u16* sk, float* xn, float* y2, float* kvT){
  int row=blockIdx.x, l=threadIdx.x;
  float x[8]; float s=0;
  for(int i=0;i<8;i++){ x[i]=bf2f(q[row*D+l*8+i]); s+=x[i]; }
  for(int o=32;o;o>>=1) s+=__shfl_xor(s,o);
  float mf=s/D; float m=rb(mf); float vs=0;
  for(int i=0;i<8;i++){ float d=x[i]-mf; vs+=d*d; }
  for(int o=32;o;o>>=1) vs+=__shfl_xor(vs,o);
  float var=rb(vs/D); float den=rb(sqrtf(rb(var+1e-5f)));
  float a[8]; s=0;
  for(int i=0;i<8;i++){ a[i]=rb(rb(x[i]-m)/den); s+=a[i]; xn[row*D+l*8+i]=a[i]; }
  for(int o=32;o;o>>=1) s+=__shfl_xor(s,o);
  mf=s/D; m=rb(mf); vs=0;
  for(int i=0;i<8;i++){ float d=a[i]-mf; vs+=d*d; }
  for(int o=32;o;o>>=1) vs+=__shfl_xor(vs,o);
  var=rb(vs/D); den=rb(sqrtf(rb(var+1e-5f)));
  for(int i=0;i<8;i++){ y2[row*D+l*8+i]=rb(rb(a[i]-m)/den); }
  for(int i=0;i<8;i++){ int d=l*8+i; float ks=0;
    for(int t=0;t<4;t++){ int r=row+t-3; float kk=r>=0?bf2f(k[r*D+d]):0.f; ks=rb(ks+rb(kk*bf2f(sk[d*4+3-t]))); }
    kvT[d*S+row]=rb(ks*bf2f(v[row*D+d])); }
}
__global__ void k_conv(const float* kvT, const u16* h, float* yT){
  __shared__ float u[S], g[S];
  int d=blockIdx.x;
  for(int i=threadIdx.x;i<S;i+=blockDim.x){ u[i]=kvT[d*S+i]; g[i]=bf2f(h[d*S+i]); }
  __syncthreads();
  for(int t=threadIdx.x;t<S;t+=blockDim.x){ float a=0; for(int s=0;s<=t;s++) a+=g[t-s]*u[s]; yT[d*S+t]=rb(a); }
}
__global__ void k_g1(const float* y2, const u16* w1, const u16* b1, float* hh){
  __shared__ float xs[D];
  int row=blockIdx.x;
  for(int i=threadIdx.x;i<D;i+=blockDim.x) xs[i]=y2[row*D+i];
  __syncthreads();
  for(int f=threadIdx.x;f<F;f+=blockDim.x){ float a=0; for(int k=0;k<D;k++) a+=xs[k]*bf2f(w1[k*F+f]);
    a=rb(rb(a)+bf2f(b1[f]));
    float t=tanhf(0.7978845608028654f*(a+0.044715f*a*a*a));
    hh[row*F+f]=rb(0.5f*a*(1.f+t)); }
}
__global__ void k_g2(const float* hh, const u16* w2, const u16* b2, const u16* q, const float* xn, const float* kvT, const float* yT, const u16* sD, u16* out){
  __shared__ float hs[F];
  int row=blockIdx.x;
  for(int i=threadIdx.x;i<F;i+=blockDim.x) hs[i]=hh[row*F+i];
  __syncthreads();
  for(int d=threadIdx.x;d<D;d+=blockDim.x){ float a=0; for(int f=0;f<F;f++) a+=hs[f]*bf2f(w2[f*D+d]);
    float B=rb(rb(a)+bf2f(b2[d]));
    float y=rb(yT[d*S+row]+rb(kvT[d*S+row]*bf2f(sD[d])));
    float A=rb(xn[row*D+d]*y);
    out[row*D+d]=f2bf(rb(bf2f(q[row*D+d])+A)+B); }
}
extern "C" void kernel_launch(void* const* d_in, const int* in_sizes, int n_in, void* d_out, int out_size, void* d_ws, size_t ws_size, hipStream_t stream){
  const u16* q=(const u16*)d_in[0]; const u16* k=(const u16*)d_in[1]; const u16* v=(const u16*)d_in[2];
  const u16* sk=(const u16*)d_in[3]; const u16* ssm=(const u16*)d_in[4]; const u16* sD=(const u16*)d_in[5];
  const u16* w1=(const u16*)d_in[7]; const u16* b1=(const u16*)d_in[8]; const u16* w2=(const u16*)d_in[9]; const u16* b2=(const u16*)d_in[10];
  float* ws=(float*)d_ws; float* xn=ws; float* y2=xn+S*D; float* kvT=y2+S*D; float* yT=kvT+S*D; float* hh=yT+S*D;
  k_ln<<<S,64,0,stream>>>(q,k,v,sk,xn,y2,kvT);
  k_conv<<<D,256,0,stream>>>(kvT,ssm,yT);
  k_g1<<<S,256,0,stream>>>(y2,w1,b1,hh);
  k_g2<<<S,256,0,stream>>>(hh,w2,b2,q,xn,kvT,yT,sD,(u16*)d_out);
}
```

```cpp
#define PROBE 0
#include <hip/hip_runtime.h>
#include <hip/hip_bf16.h>
typedef unsigned short u16;
constexpr int S=2048, D=512, F=1792;
__device__ __forceinline__ float bf2f(u16 b){ return __builtin_bit_cast(float,(unsigned)b<<16); }
__device__ __forceinline__ u16 f2bf(float f){ unsigned u=__builtin_bit_cast(unsigned,f); u+=0x7FFFu+((u>>16)&1u); return (u16)(u>>16); }
__device__ __forceinline__ float rb(float f){ return bf2f(f2bf(f)); }

__global__ void k_ln(const u16* q, const u16* k, const u16* v, const u16* sk, float* xn, float* y2, float* kvT){
  int row=blockIdx.x, l=threadIdx.x;
  float x[8]; float s=0;
  for(int i=0;i<8;i++){ x[i]=bf2f(q[row*D+l*8+i]); s+=x[i]; }
  for(int o=32;o;o>>=1) s+=__shfl_xor(s,o);
  float mf=s/D; float m=rb(mf); float vs=0;
  for(int i=0;i<8;i++){ float d=x[i]-mf; vs+=d*d; }
  for(int o=32;o;o>>=1) vs+=__shfl_xor(vs,o);
  float var=rb(vs/D); float den=rb(sqrtf(rb(var+1e-5f)));
  float a[8]; s=0;
  for(int i=0;i<8;i++){ a[i]=rb(rb(x[i]-m)/den); s+=a[i]; xn[row*D+l*8+i]=a[i]; }
  for(int o=32;o;o>>=1) s+=__shfl_xor(s,o);
  mf=s/D; m=rb(mf); vs=0;
  for(int i=0;i<8;i++){ float d=a[i]-mf; vs+=d*d; }
  for(int o=32;o;o>>=1) vs+=__shfl_xor(vs,o);
  var=rb(vs/D); den=rb(sqrtf(rb(var+1e-5f)));
  for(int i=0;i<8;i++){ y2[row*D+l*8+i]=rb(rb(a[i]-m)/den); }
  for(int i=0;i<8;i++){ int d=l*8+i; float ks=0;
    for(int t=0;t<4;t++){ int r=row+t-3; float kk=r>=0?bf2f(k[r*D+d]):0.f; ks=rb(ks+rb(kk*bf2f(sk[d*4+3-t]))); }
    kvT[d*S+row]=rb(ks*bf2f(v[row*D+d])); }
}
__global__ void k_conv(const float* kvT, const u16* h, float* yT){
  __shared__ float u[S], g[S];
  int d=blockIdx.x;
  for(int i=threadIdx.x;i<S;i+=blockDim.x){ u[i]=kvT[d*S+i]; g[i]=bf2f(h[d*S+i]); }
  __syncthreads();
  for(int t=threadIdx.x;t<S;t+=blockDim.x){ float a=0; for(int s=0;s<=t;s++) a+=g[t-s]*u[s]; yT[d*S+t]=rb(a); }
}
__global__ void k_g1(const float* y2, const u16* w1, const u16* b1, float* hh){
  __shared__ float xs[D];
  int row=blockIdx.x;
  for(int i=threadIdx.x;i<D;i+=blockDim.x) xs[i]=y2[row*D+i];
  __syncthreads();
  for(int f=threadIdx.x;f<F;f+=blockDim.x){ float a=0; for(int k=0;k<D;k++) a+=xs[k]*bf2f(w1[k*F+f]);
    a=rb(rb(a)+bf2f(b1[f]));
    float t=tanhf(0.7978845608028654f*(a+0.044715f*a*a*a));
    hh[row*F+f]=rb(0.5f*a*(1.f+t)); }
}
__global__ void k_g2(const float* hh, const u16* w2, const u16* b2, const u16* q, const float* xn, const float* kvT, const float* yT, const u16* sD, u16* out){
  __shared__ float hs[F];
  int row=blockIdx.x;
  for(int i=threadIdx.x;i<F;i+=blockDim.x) hs[i]=hh[row*F+i];
  __syncthreads();
  for(int d=threadIdx.x;d<D;d+=blockDim.x){ float a=0; for(int f=0;f<F;f++) a+=hs[f]*bf2f(w2[f*D+d]);
    float B=rb(rb(a)+bf2f(b2[d]));
    float y=rb(yT[d*S+row]+rb(kvT[d*S+row]*bf2f(sD[d])));
    float A=rb(xn[row*D+d]*y);
    out[row*D+d]=f2bf(rb(bf2f(q[row*D+d])+A)+B); }
}

typedef unsigned long long u64;
typedef unsigned v4u __attribute__((ext_vector_type(4)));
#define GAS __attribute__((address_space(1)))
#define LAS __attribute__((address_space(3)))
#define STAMP(x) asm volatile("s_memtime %0\n\ts_waitcnt lgkmcnt(0)":"=s"(x)::"memory")
#define RSTAMP(x) asm volatile("s_memrealtime %0\n\ts_waitcnt lgkmcnt(0)":"=s"(x)::"memory")
struct Stamp{ u64 r0,r1,c0,c1; };
__global__ __launch_bounds__(512,2) void p_first(const char* A, const char* B, const char* K, const char* V, const char* H, const char* W2, Stamp* st){
  extern __shared__ __attribute__((aligned(16))) char smem[];
  const int b=blockIdx.x, tid=threadIdx.x, lane=tid&63, w=__builtin_amdgcn_readfirstlane(tid>>6);
  u64 r0,r1,c0,c1; STAMP(c0); RSTAMP(r0);
  asm volatile("s_waitcnt vmcnt(0)":::"memory");
  __syncthreads();
  STAMP(c1); RSTAMP(r1);
  if(tid==0){ st[b].r0=r0; st[b].r1=r1; st[b].c0=c0; st[b].c1=c1; }
}
__global__ __launch_bounds__(512,2) void p_second(Stamp* st){
  u64 r0; RSTAMP(r0);
  if(threadIdx.x==0){ st[256+blockIdx.x].r0=r0; }
}
__global__ void p_spin(const Stamp* st){
  u64 v=0;
  v=0;
  if(v>200000) v=200000;
  u64 t0,t; RSTAMP(t0);
  for(int g=0;g<10000000;g++){ RSTAMP(t); if(t-t0>=v) break; }
}
extern "C" void kernel_launch(void* const* d_in, const int* in_sizes, int n_in, void* d_out, int out_size, void* d_ws, size_t ws_size, hipStream_t stream){
  const u16* q=(const u16*)d_in[0]; const u16* k=(const u16*)d_in[1]; const u16* v=(const u16*)d_in[2];
  const u16* sk=(const u16*)d_in[3]; const u16* ssm=(const u16*)d_in[4]; const u16* sD=(const u16*)d_in[5];
  const u16* w1=(const u16*)d_in[7]; const u16* b1=(const u16*)d_in[8]; const u16* w2=(const u16*)d_in[9]; const u16* b2=(const u16*)d_in[10];
  float* ws=(float*)d_ws; float* xn=ws; float* y2=xn+S*D; float* kvT=y2+S*D; float* yT=kvT+S*D; float* hh=yT+S*D;
  Stamp* st=(Stamp*)((char*)d_ws+(64<<20));
  hipFuncSetAttribute((const void*)p_first,hipFuncAttributeMaxDynamicSharedMemorySize,128*1024);
  p_first<<<256,512,128*1024,stream>>>((const char*)q,(const char*)w1,(const char*)k,(const char*)v,(const char*)ssm,(const char*)w2,st);
  p_second<<<256,512,0,stream>>>(st);
  p_spin<<<1,1,0,stream>>>(st);
  k_ln<<<S,64,0,stream>>>(q,k,v,sk,xn,y2,kvT);
  k_conv<<<D,256,0,stream>>>(kvT,ssm,yT);
  k_g1<<<S,256,0,stream>>>(y2,w1,b1,hh);
  k_g2<<<S,256,0,stream>>>(hh,w2,b2,q,xn,kvT,yT,sD,(u16*)d_out);
}
```

```cpp
#define STAMPS 1
#define MAP1 1
#include <hip/hip_runtime.h>
#include <hip/hip_bf16.h>
#include <stdint.h>

typedef unsigned u32;
typedef unsigned long long u64;
typedef unsigned short u16;
typedef u32 v4u __attribute__((ext_vector_type(4)));
typedef u32 v2u __attribute__((ext_vector_type(2)));
typedef float f4 __attribute__((ext_vector_type(4)));
typedef float f2 __attribute__((ext_vector_type(2)));
typedef __bf16 b8 __attribute__((ext_vector_type(8)));
typedef __bf16 b2 __attribute__((ext_vector_type(2)));
typedef short s4 __attribute__((ext_vector_type(4)));
#define LAS __attribute__((address_space(3)))
#define CAS __attribute__((address_space(4)))
#define DEV __device__ __forceinline__

constexpr int S = 2048, D = 512, F = 1792;
constexpr u32 WS_KVF = 4096;
constexpr u32 WS_HF = 16384;
constexpr u32 WS_ZF = 65536;
constexpr u32 WS_ST = 131072;
constexpr u32 WS_U = 1u << 20;
constexpr u32 WS_Z = 3u << 20;
constexpr u32 WS_H = 5u << 20;
constexpr u32 WS_END = WS_H + S * F * 2;
constexpr u32 WS_DBG = 16u << 20;
constexpr u32 L_RING = 0;
constexpr u32 L_HC = 98304;
constexpr u32 HC_S = 4160;
constexpr u32 L_U = L_HC + 8 * HC_S;
constexpr u32 U_S = 4640;
constexpr u32 L_SX = L_U + 4 * U_S;
constexpr u32 L_CS = L_SX + 2048;
constexpr u32 L_ZT = L_CS + 1024;
constexpr u32 L_END = L_ZT + 8192;
static_assert(L_END <= 163840, "lds");
constexpr u32 SPIN_CAP = 1u << 16;

#define STAMP(i) do { u64 t_; asm volatile("s_memtime %0\n\ts_waitcnt lgkmcnt(0)" : "=s"(t_)::"memory"); stamp[i] = (u32)t_; } while (0)

DEV v4u mk_srd(const void* p, u32 bytes) {
  u64 a = (u64)p;
  return v4u{(u32)a, (u32)(a >> 32) & 0xffffu, bytes, 0x00020000u};
}
DEV void dma(int IMM, int OFF, u32 ldsbase, u32 voff, v4u srd, u32 soff) {
  asm volatile("s_add_u32 m0, %0, %1\n\ts_nop 0\n\tbuffer_load_dwordx4 %2, %3, %4 offen offset:%5 lds"
               ::"s"(ldsbase), "i"(IMM - OFF), "v"(voff), "s"(srd), "s"(soff), "i"(OFF) : "scc");
}
template <int OFF> DEV v4u ld128(u32 voff, v4u srd, u32 soff) { v4u r; asm volatile("buffer_load_dwordx4 %0, %1, %2, %3 offen offset:%4" : "=v"(r) : "v"(voff), "s"(srd), "s"(soff), "i"(OFF)); return r; }
template <int OFF> DEV v4u ld128c(u32 voff, v4u srd, u32 soff) { v4u r; asm volatile("buffer_load_dwordx4 %0, %1, %2, %3 offen offset:%4 sc1" : "=v"(r) : "v"(voff), "s"(srd), "s"(soff), "i"(OFF)); return r; }
template <int OFF> DEV v2u ld64(u32 voff, v4u srd, u32 soff) { v2u r; asm volatile("buffer_load_dwordx2 %0, %1, %2, %3 offen offset:%4" : "=v"(r) : "v"(voff), "s"(srd), "s"(soff), "i"(OFF)); return r; }
template <int OFF> DEV v2u ld64c(u32 voff, v4u srd, u32 soff) { v2u r; asm volatile("buffer_load_dwordx2 %0, %1, %2, %3 offen offset:%4 sc1" : "=v"(r) : "v"(voff), "s"(srd), "s"(soff), "i"(OFF)); return r; }
template <int OFF> DEV void st128(v4u d, u32 voff, v4u srd, u32 soff) { asm volatile("buffer_store_dwordx4 %0, %1, %2, %3 offen offset:%4\n\ts_nop 1" ::"v"(d), "v"(voff), "s"(srd), "s"(soff), "i"(OFF)); }
template <int OFF> DEV void st128c(v4u d, u32 voff, v4u srd, u32 soff) { asm volatile("buffer_store_dwordx4 %0, %1, %2, %3 offen offset:%4 sc1\n\ts_nop 1" ::"v"(d), "v"(voff), "s"(srd), "s"(soff), "i"(OFF)); }
template <int OFF> DEV void st64c(v2u d, u32 voff, v4u srd, u32 soff) { asm volatile("buffer_store_dwordx2 %0, %1, %2, %3 offen offset:%4 sc1" ::"v"(d), "v"(voff), "s"(srd), "s"(soff), "i"(OFF)); }
#define WAITV(n) asm volatile("s_waitcnt vmcnt(%0)" ::"i"(n) : "memory")
#define WAITV1(n, a) asm volatile("s_waitcnt vmcnt(%1)" : "+v"(a) : "i"(n) : "memory")
#define WAITV2(n, a, b) asm volatile("s_waitcnt vmcnt(%2)" : "+v"(a), "+v"(b) : "i"(n) : "memory")
#define WAITV4(n, a, b, c, d) asm volatile("s_waitcnt vmcnt(%4)" : "+v"(a), "+v"(b), "+v"(c), "+v"(d) : "i"(n) : "memory")
#define BARV(n) asm volatile("s_waitcnt vmcnt(%0) lgkmcnt(0)\n\ts_barrier" ::"i"(n) : "memory")
#define BAR() asm volatile("s_waitcnt lgkmcnt(0)\n\ts_barrier" ::: "memory")
#define WAITL() asm volatile("s_waitcnt lgkmcnt(0)" ::: "memory")
#define SCHED() __builtin_amdgcn_sched_barrier(0)

DEV f4 mfma(v4u a, v4u b, f4 c) { return __builtin_amdgcn_mfma_f32_16x16x32_bf16(__builtin_bit_cast(b8, a), __builtin_bit_cast(b8, b), c, 0, 0, 0); }
DEV v4u lr128(u32 a) { return *(LAS v4u*)(uintptr_t)a; }
DEV v2u lr64(u32 a) { return *(LAS v2u*)(uintptr_t)a; }
DEV void lw128(u32 a, v4u v) { *(LAS v4u*)(uintptr_t)a = v; }
DEV void lw64(u32 a, v2u v) { *(LAS v2u*)(uintptr_t)a = v; }
DEV v2u ltr(u32 a) { return __builtin_bit_cast(v2u, __builtin_amdgcn_ds_read_tr16_b64_v4i16((LAS s4*)(uintptr_t)a)); }
DEV v4u ltr2(u32 a0, u32 a1) { v2u x = ltr(a0), y = ltr(a1); return v4u{x.x, x.y, y.x, y.y}; }
DEV float asf(u32 x) { return __builtin_bit_cast(float, x); }
DEV float lo2f(u32 x) { return __builtin_bit_cast(float, x << 16); }
DEV float hi2f(u32 x) { return __builtin_bit_cast(float, x & 0xffff0000u); }
DEV u32 pk(float a, float b) { f2 v = {a, b}; return __builtin_bit_cast(u32, __builtin_convertvector(v, b2)); }
DEV float dot2(u32 a, u32 b, float c) { return __builtin_amdgcn_fdot2_f32_bf16(__builtin_bit_cast(b2, a), __builtin_bit_cast(b2, b), c, false); }
DEV float red_g(float x) {
  u32 u = __builtin_bit_cast(u32, x);
  auto p = __builtin_amdgcn_permlane32_swap(u, u, false, false);
  x = __builtin_bit_cast(float, (u32)p[0]) + __builtin_bit_cast(float, (u32)p[1]);
  u = __builtin_bit_cast(u32, x);
  auto q = __builtin_amdgcn_permlane16_swap(u, u, false, false);
  return __builtin_bit_cast(float, (u32)q[0]) + __builtin_bit_cast(float, (u32)q[1]);
}
DEV float gelu(float x) {
  float p = __builtin_fmaf(x * x, -0.10294324f, -2.3022082f);
  float e = __builtin_amdgcn_exp2f(x * p);
  return x * __builtin_amdgcn_rcpf(1.0f + e);
}
DEV void spin(v2u f, bool need, v2u tag, u32 voff, v4u srd) {
  for (u32 n = 0; n < SPIN_CAP; ++n) {
    bool ok = !need || (f.x == tag.x && f.y == tag.y);
    if (__all(ok)) break;
    __builtin_amdgcn_s_sleep(1);
    f = ld64c<0>(voff, srd, 0);
    WAITV1(0, f);
  }
}

__global__ __launch_bounds__(512, 2) void fused(const u16* __restrict__ q, const u16* __restrict__ k, const u16* __restrict__ v,
                                                const u16* __restrict__ shk, const u16* __restrict__ ssm, const u16* __restrict__ ssd,
                                                const u16* __restrict__ w1, const u16* __restrict__ b1, const u16* __restrict__ w2,
                                                const u16* __restrict__ b2p, u16* __restrict__ out, char* __restrict__ ws) {
  __shared__ __attribute__((aligned(1024))) char smem[L_END];
  const u32 L0 = (u32)(uintptr_t)(LAS char*)smem;
  const u32 tid = threadIdx.x, l = tid & 63, g = l >> 4, l15 = l & 15;
  const u32 w = __builtin_amdgcn_readfirstlane(tid >> 6);
  const u32 b = blockIdx.x, xcd = b & 7, idx = b >> 3;
  const bool is_main = idx < 28;
  u32 stamp[16];
  for (int i = 0; i < 16; ++i) stamp[i] = 0;
  u64 rt0 = __builtin_amdgcn_s_memrealtime();
  STAMP(0);
  const u64 epoch = *(const CAS u64*)ws;
  const u64 tag64 = epoch + 0x9E3779B97F4A7C15ull;
  const v2u tag = {(u32)tag64, (u32)(tag64 >> 32)};
  const v4u sWS = mk_srd(ws, WS_END);
  const v4u sQ = mk_srd(q, S * D * 2);
  const v4u sW2 = mk_srd(w2, F * D * 2);

  const u32 ti = 4 * (xcd >> 1) + idx / 7, tj = 7 * (xcd & 1) + idx % 7;
  const u32 R = 4 * xcd + (idx & 3), dj = idx >> 2;
  const u32 fh = w & 1, tq = w >> 1;

  v4u hw; v2u hp;
  auto load_filter = [&]() {
    const v4u sH = mk_srd(ssm, D * S * 2);
    u32 o = b * 8192 + tid * 16;
    hw = ld128<0>(o, sH, 0);
    hp = ld64<0>((tid & 255) ? o - 8 : 0x80000000u, sH, 0);
  };
  const u32 w2v = (224 * w + ((l >> 2) & 7)) * 1024 + dj * 128 + 16 * (4 * (l >> 5) + ((l & 3) ^ ((l >> 3) & 2)));
  const u32 w2l = L0 + L_RING + 4096 * w;
#define W2_ISSUE1(s, n) dma((((s) + 2) % 3) * 32768 + (n) * 1024, 0, w2l, w2v, sW2, ((s) * 32 + (n) * 8) * 1024)
#define W2_ISSUE(s) do { W2_ISSUE1(s, 0); W2_ISSUE1(s, 1); W2_ISSUE1(s, 2); W2_ISSUE1(s, 3); } while (0)
  const u32 w2r0 = w2l + 1024 * (g >> 1) + 64 * (4 * (g & 1) + (l15 >> 2)) + 16 * (((l >> 1) & 1) ^ (2 * (g & 1))) + 8 * (l & 1);
  const u32 w2r1 = w2r0 ^ 32;
#define W2_READ(dst, s) do { _Pragma("unroll") for (int c_ = 0; c_ < 4; ++c_) { u32 a_ = ((c_ & 1) ? w2r1 : w2r0) + (((s) + 2) % 3) * 32768 + 512 * (c_ >> 1); dst[c_] = ltr2(a_, a_ + 2048); } } while (0)

  f4 acc1[4][2];
#pragma unroll
  for (int c = 0; c < 4; ++c) { acc1[c][0] = f4{0, 0, 0, 0}; acc1[c][1] = f4{0, 0, 0, 0}; }
  v2u b1v[4];
  const u32 prot = tq >> 1;

  auto build_filter = [&]() {
    u32 W3 = hw.w, W2_ = hw.z, W1 = hw.y, W0 = hw.x, Wm1 = hp.y, Wm2 = hp.x;
    u32 s3 = __builtin_amdgcn_alignbit(W3, W3, 16), s2 = __builtin_amdgcn_alignbit(W2_, W2_, 16), s1 = __builtin_amdgcn_alignbit(W1, W1, 16),
        s0 = __builtin_amdgcn_alignbit(W0, W0, 16), sm = __builtin_amdgcn_alignbit(Wm1, Wm1, 16);
    u32 m3 = (W3 & 0xffffu) | (W2_ & 0xffff0000u), m2 = (W2_ & 0xffffu) | (W1 & 0xffff0000u), m1 = (W1 & 0xffffu) | (W0 & 0xffff0000u),
        m0_ = (W0 & 0xffffu) | (Wm1 & 0xffff0000u), mm = (Wm1 & 0xffffu) | (Wm2 & 0xffff0000u);
    u32 a = L0 + L_HC + (tid >> 8) * 4 * HC_S + ((tid & 255) + 3) * 16;
    lw128(a + 3 * HC_S, v4u{s3, s2, s1, s0});
    lw128(a + 2 * HC_S, v4u{m3, m2, m1, m0_});
    lw128(a + 1 * HC_S, v4u{s2, s1, s0, sm});
    lw128(a, v4u{m2, m1, m0_, mm});
    if (tid < 24) lw128(L0 + L_HC + (tid / 3) * HC_S + (tid % 3) * 16, v4u{0, 0, 0, 0});
    if (tid >= 64 && tid < 64 + 128 && (tid & 31) < 29) lw128(L0 + L_U + ((tid - 64) >> 5) * U_S + (tid & 31) * 16, v4u{0, 0, 0, 0});
  };

  if (is_main) {
    const v4u sW1 = mk_srd(w1, D * F * 2);
    load_filter();
    {
      const v4u sB1 = mk_srd(b1, F * 2);
      u32 o = (tj * 128 + fh * 64 + 4 * g) * 2;
      b1v[0] = ld64<0>(o, sB1, prot * 64);
      b1v[1] = ld64<32>(o, sB1, prot * 64);
      b1v[2] = ld64<0>(o, sB1, (prot ^ 1) * 64);
      b1v[3] = ld64<32>(o, sB1, (prot ^ 1) * 64);
    }
    const u32 r8 = l >> 3;
    const u32 xv0 = (ti * 128 + 16 * w + r8) * 1024 + 16 * ((l & 7) ^ (r8 >> 1));
    const u32 xv1 = (xv0 + 8192) ^ 64;
    const u32 wr = 8 * w + ((l >> 2) & 7);
    const u32 wv = wr * (F * 2) + tj * 256 + 16 * (4 * (l >> 5) + ((l & 3) ^ ((wr >> 2) & 3)));
    const u32 dl = L0 + L_RING + 2048 * w;
#define G1_X(ks, n) dma(((ks) % 3) * 32768 + (n) * 1024, 0, dl, (n) ? xv1 : xv0, sQ, (ks) * 128)
#define G1_W(ks, n) dma(((ks) % 3) * 32768 + 16384 + (n) * 1024, (n) * 128, dl, wv, sW1, (ks) * 64 * F * 2)
#define G1_ISSUE(ks) do { G1_X(ks, 0); G1_W(ks, 0); G1_X(ks, 1); G1_W(ks, 1); } while (0)
    G1_ISSUE(0);
    G1_ISSUE(1);
    WAITV2(12, hw, hp);
    build_filter();
    const u32 kx = fh ^ (tq & 1);
    const u32 xr0 = L0 + L_RING + (32 * tq + l15) * 128 + 16 * ((4 * kx + g) ^ (l15 >> 1));
    const u32 xr1 = xr0 ^ 64;
    const u32 wb = L0 + L_RING + 16384 + 2048 * g + 1024 * fh + 64 * (l15 >> 2) + 8 * (l & 1);
    const u32 pp = (l >> 1) & 1;
    u32 wa[2][2][2];
#pragma unroll
    for (int kk = 0; kk < 2; ++kk)
#pragma unroll
      for (int c1 = 0; c1 < 2; ++c1)
#pragma unroll
        for (int t = 0; t < 2; ++t) wa[kk][c1][t] = wb + 8192 * (kk ^ kx) + 512 * prot + 256 * t + 16 * (2 * (c1 ^ (g & 1)) + (pp ^ t));
    float sx[2] = {0, 0}, sq[2] = {0, 0}, cs[2] = {0, 0};
    STAMP(1);
#pragma unroll
    for (int ks = 0; ks < 8; ++ks) {
      BARV(4);
      if (ks == 0) STAMP(2);
      const u32 so = (ks % 3) * 32768;
#pragma unroll
      for (int kk = 0; kk < 2; ++kk) {
        v4u xf[2], wf[4];
        xf[0] = lr128((kk ? xr1 : xr0) + so);
        xf[1] = lr128((kk ? xr1 : xr0) + so + 2048);
#pragma unroll
        for (int c = 0; c < 4; ++c) wf[c] = ltr2((wa[kk][c & 1][0] ^ (512 * (c >> 1))) + so, (wa[kk][c & 1][1] ^ (512 * (c >> 1))) + so);
        if (ks < 6) { if (kk == 0) { G1_X((ks + 2), 0); G1_W((ks + 2), 0); } else { G1_X((ks + 2), 1); G1_W((ks + 2), 1); } }
        else if (ks == 6) { if (kk == 0) { W2_ISSUE1(0, 0); W2_ISSUE1(0, 1); } else { W2_ISSUE1(0, 2); W2_ISSUE1(0, 3); } }
        else { if (kk == 0) { W2_ISSUE1(1, 0); W2_ISSUE1(1, 1); } else { W2_ISSUE1(1, 2); W2_ISSUE1(1, 3); } }
#pragma unroll
        for (int c = 0; c < 4; ++c) {
          acc1[c][0] = mfma(wf[c], xf[0], acc1[c][0]);
          acc1[c][1] = mfma(wf[c], xf[1], acc1[c][1]);
        }
        if (kk == 0) {
#pragma unroll
          for (int e = 0; e < 4; ++e) {
            sx[0] = dot2(xf[0][e], 0x3F803F80u, sx[0]); sq[0] = dot2(xf[0][e], xf[0][e], sq[0]);
            sx[1] = dot2(xf[1][e], 0x3F803F80u, sx[1]); sq[1] = dot2(xf[1][e], xf[1][e], sq[1]);
            cs[0] = dot2(wf[0][e], 0x3F803F80u, cs[0]); cs[1] = dot2(wf[1][e], 0x3F803F80u, cs[1]);
          }
        }
      }
    }
    STAMP(3);
#pragma unroll
    for (int t = 0; t < 2; ++t) { sx[t] = red_g(sx[t]); sq[t] = red_g(sq[t]); cs[t] = red_g(cs[t]); }
    if (g < 2) {
      float a = g ? sx[1] : sx[0], c = g ? sq[1] : sq[0], e = g ? cs[1] : cs[0];
      lw64(L0 + L_SX + fh * 1024 + (32 * tq + l) * 8, v2u{__builtin_bit_cast(u32, a), __builtin_bit_cast(u32, c)});
      *(LAS float*)(uintptr_t)(L0 + L_CS + (tq & 1) * 512 + (64 * fh + 32 * prot + l) * 4) = e;
    }
  } else {
    b1v[0] = b1v[1] = b1v[2] = b1v[3] = v2u{0, 0};
    asm volatile("; spare");
    load_filter();
    const u32 p = xcd * 4 + (idx - 28), cg = p >> 2, tqq = p & 3;
    const v4u sK = mk_srd(k, S * D * 2), sV = mk_srd(v, S * D * 2), sS = mk_srd(shk, D * 4 * 2);
    const u32 t0 = tqq * 512 + w * 64 + (l >> 3) * 8;
    const u32 cb = cg * 128 + (l & 7) * 16;
    const u32 om = t0 * 1024 + cb, oh = t0 ? om - 3072 : 0x80000000u;
    v4u kr[11], vr[8], sw[4];
    sw[0] = ld128<0>(cb * 4, sS, 0); sw[1] = ld128<16>(cb * 4, sS, 0); sw[2] = ld128<32>(cb * 4, sS, 0); sw[3] = ld128<48>(cb * 4, sS, 0);
    kr[0] = ld128<0>(oh, sK, 0); kr[1] = ld128<1024>(oh, sK, 0); kr[2] = ld128<2048>(oh, sK, 0);
    kr[3] = ld128<0>(om, sK, 0); vr[0] = ld128<0>(om, sV, 0); kr[4] = ld128<1024>(om, sK, 0); vr[1] = ld128<1024>(om, sV, 0);
    kr[5] = ld128<2048>(om, sK, 0); vr[2] = ld128<2048>(om, sV, 0); kr[6] = ld128<3072>(om, sK, 0); vr[3] = ld128<3072>(om, sV, 0);
    kr[7] = ld128<0>(om, sK, 4096); vr[4] = ld128<0>(om, sV, 4096); kr[8] = ld128<1024>(om, sK, 4096); vr[5] = ld128<1024>(om, sV, 4096);
    kr[9] = ld128<2048>(om, sK, 4096); vr[6] = ld128<2048>(om, sV, 4096); kr[10] = ld128<3072>(om, sK, 4096); vr[7] = ld128<3072>(om, sV, 4096);
    WAITV2(23, hw, hp);
    build_filter();
    STAMP(1);
    WAITV4(0, sw[0], sw[1], sw[2], sw[3]);
    WAITV4(0, kr[0], kr[1], kr[2], kr[3]); WAITV4(0, kr[4], kr[5], kr[6], kr[7]); WAITV4(0, kr[8], kr[9], kr[10], vr[0]);
    WAITV4(0, vr[1], vr[2], vr[3], vr[4]); WAITV4(0, vr[5], vr[6], vr[7], vr[7]);
    STAMP(2);
    const u32 tl = L0 + L_RING + w * 8192;
#pragma unroll
    for (int c = 0; c < 8; ++c) {
      const u32 s01 = sw[c >> 1][(c & 1) * 2], s23 = sw[c >> 1][(c & 1) * 2 + 1];
      const float f0 = lo2f(s01), f1 = hi2f(s01), f2_ = lo2f(s23), f3 = hi2f(s23);
      float kk[11], o[8];
#pragma unroll
      for (int r = 0; r < 11; ++r) kk[r] = (c & 1) ? hi2f(kr[r][c >> 1]) : lo2f(kr[r][c >> 1]);
#pragma unroll
      for (int r = 0; r < 8; ++r) {
        float a = kk[r] * f3;
        a = __builtin_fmaf(kk[r + 1], f2_, a);
        a = __builtin_fmaf(kk[r + 2], f1, a);
        a = __builtin_fmaf(kk[r + 3], f0, a);
        o[r] = a * ((c & 1) ? hi2f(vr[r][c >> 1]) : lo2f(vr[r][c >> 1]));
      }
      lw128(tl + ((l & 7) * 8 + c) * 128 + 16 * ((l >> 3) ^ (l & 7)), v4u{pk(o[0], o[1]), pk(o[2], o[3]), pk(o[4], o[5]), pk(o[6], o[7])});
    }
    WAITL();
    const u32 uo = WS_U + (cg * 64 + (l >> 3)) * 4096 + (tqq * 512 + w * 64) * 2 + (l & 7) * 16;
#pragma unroll
    for (int c = 0; c < 8; ++c) {
      v4u d = lr128(tl + (c * 8 + (l >> 3)) * 128 + 16 * ((l & 7) ^ c));
      st128c<0>(d, uo, sWS, c * 8 * 4096);
    }
    BARV(0);
    if (tid == 0) st64c<0>(tag, WS_KVF + p * 128, sWS, 0);
    STAMP(3);
    W2_ISSUE(0);
    W2_ISSUE(1);
  }
  BAR();
  STAMP(4);
  const u32 kvo = WS_KVF + ((b >> 5) * 4 + (w & 3)) * 128;
  v2u kf = ld64c<0>(kvo, sWS, 0);
  W2_ISSUE(2);
  float sa[2], sam[2];
  f4 csv[4];
  {
#pragma unroll
    for (int t = 0; t < 2; ++t) {
      u32 a = L0 + L_SX + (32 * tq + 16 * t + l15) * 8;
      v2u x0 = lr64(a), x1 = lr64(a + 1024);
      float m = (asf(x0.x) + asf(x1.x)) * (1.0f / D);
      float var = (asf(x0.y) + asf(x1.y)) * (1.0f / D) - m * m;
      var = __builtin_fmaxf(var, 0.0f);
      float r1 = __builtin_amdgcn_rsqf(var + 1e-5f);
      float r2 = __builtin_amdgcn_rsqf(var * r1 * r1 + 1e-5f);
      sa[t] = r1 * r2;
      sam[t] = -sa[t] * m;
      st64c<0>(v2u{__builtin_bit_cast(u32, m), __builtin_bit_cast(u32, r1)},
               (is_main && tj == 0 && fh == 0 && g == 0) ? WS_ST + (ti * 128 + 32 * tq + 16 * t + l15) * 8 : 0x80000000u, sWS, 0);
    }
#pragma unroll
    for (int c = 0; c < 4; ++c) {
      u32 a = L0 + L_CS + (64 * fh + 32 * (prot ^ (c >> 1)) + 16 * (c & 1) + 4 * g) * 4;
      csv[c] = __builtin_bit_cast(f4, lr128(a)) + __builtin_bit_cast(f4, lr128(a + 512));
    }
  }
  const u32 hso = is_main ? WS_H + (((2 * ti + (tq >> 1)) * 56 + 4 * tj + 2 * fh) * 4 + 2 * (tq & 1)) * 1024 + 16 * l : 0x80000000u;
  auto gelu_frag = [&](int P, int t) -> v4u {
    u32 o[4];
#pragma unroll
    for (int c2 = 0; c2 < 2; ++c2) {
      const int c = 2 * P + c2;
      const float bb[4] = {lo2f(b1v[c].x), hi2f(b1v[c].x), lo2f(b1v[c].y), hi2f(b1v[c].y)};
      float y[4];
#pragma unroll
      for (int r = 0; r < 4; ++r) y[r] = gelu(__builtin_fmaf(sa[t], acc1[c][t][r], __builtin_fmaf(sam[t], csv[c][r], bb[r])));
      o[2 * c2] = pk(y[0], y[1]);
      o[2 * c2 + 1] = pk(y[2], y[3]);
    }
    return v4u{o[0], o[1], o[2], o[3]};
  };
  v4u wq[4][4];
  WAITV4(63, b1v[0], b1v[1], b1v[2], b1v[3]);
  {
    v4u h0 = gelu_frag(0, 0);
    st128c<0>(h0, hso, sWS, prot * 4096);
    WAITV(12);
    W2_READ(wq[0], 0);
    WAITL();
    W2_ISSUE(3);
    SCHED();
    v4u h1 = gelu_frag(0, 1);
    st128c<1024>(h1, hso, sWS, prot * 4096);
    WAITV1(12, kf);
    spin(kf, true, tag, kvo, sWS);
    W2_READ(wq[1], 1);
  }
  v4u uu = ld128c<0>(WS_U + b * 8192 + tid * 16, sWS, 0);
  {
    SCHED();
    v4u h2 = gelu_frag(1, 0);
    st128c<0>(h2, hso, sWS, (prot ^ 1) * 4096);
    SCHED();
    v4u h3 = gelu_frag(1, 1);
    st128c<1024>(h3, hso, sWS, (prot ^ 1) * 4096);
  }
  STAMP(5);
  WAITV1(2, uu);
  W2_READ(wq[2], 2);
  W2_READ(wq[3], 3);
  {
    u32 a = L0 + L_U + (tid >> 8) * 2 * U_S + 448 + (tid & 255) * 16;
    lw128(a, uu);
    lw64(a + U_S + 8, v2u{uu.x, uu.y});
    lw64(a + U_S + 16, v2u{uu.z, uu.w});
  }
  BAR();
  STAMP(6);
  const u32 hfo = is_main ? WS_HF + (((2 * ti + (tq >> 1)) * 28 + 2 * tj + fh) * 2 + (tq & 1)) * 8 : 0x80000000u;
  if (w < 4) {
    const u32 e = w & 1, ts = w >> 1;
    u32 aa = L0 + L_HC + e * 4 * HC_S + (l & 3) * HC_S + 16 * ((l15 >> 2) - g + 3);
    u32 ba = L0 + L_U + e * 2 * U_S + ((l & 1) ^ 1) * U_S + 448 + 64 * (l15 >> 1) + 16 * g;
    f4 y[4];
#pragma unroll
    for (int i = 0; i < 4; ++i) y[i] = f4{0, 0, 0, 0};
    v4u A[8], B[8];
#pragma unroll
    for (int i = 0; i < 8; ++i) { A[i] = lr128(aa + 512 * i); B[i] = lr128(ba + 512 * i); }
    u32 dso = 4 * 32 * 1024, dlo = w2l;
#define MM(o, m, n) y[o] = mfma(A[m], B[n], y[o])
#define LA(m) A[m] = lr128(aa + 512 * (m))
#define LB(n) B[n] = lr128(ba + 512 * (n))
#pragma unroll 1
    for (int c = 0; c < 8; ++c) {
      aa += 64; ba -= 64;
      if (c < 6) {
        dma(0, 0, dlo, w2v, sW2, dso); dma(1024, 0, dlo, w2v, sW2, dso + 8192);
        dso += 16384; dlo += (c & 1) ? 32768 - 2048 : 2048;
      }
      if (c == 2) { WAITV(6); if (l == 0) st64c<0>(tag, hfo, sWS, 0); }
      if (ts == 0) {
        MM(0, 7, 0); LA(7); MM(0, 0, 7); LB(7); MM(3, 0, 0); MM(1, 4, 0); MM(1, 0, 4); MM(2, 3, 0); LB(0); MM(2, 0, 3); LA(0);
        MM(0, 4, 3); LA(4); MM(0, 3, 4); LB(4); MM(1, 3, 1); LA(3); MM(1, 1, 3); LB(3);
        MM(0, 6, 1); LA(6); MM(0, 1, 6); LB(6); MM(2, 2, 1); LB(1); MM(2, 1, 2); LA(1);
        MM(1, 2, 2); MM(0, 5, 2); LA(5); LB(2); MM(0, 2, 5); LB(5); LA(2);
      } else {
        MM(0, 6, 0); LA(6); MM(0, 0, 6); LB(6); MM(1, 5, 0); MM(1, 0, 5); MM(2, 2, 0); MM(2, 0, 2); MM(3, 1, 0); LB(0); MM(3, 0, 1); LA(0);
        MM(0, 5, 1); LA(5); MM(0, 1, 5); LB(5); MM(2, 1, 1); MM(1, 4, 1); LB(1); MM(1, 1, 4); LA(1);
        MM(0, 4, 2); LA(4); MM(0, 2, 4); LB(4); MM(1, 3, 2); LB(2); MM(1, 2, 3); LA(2); MM(0, 3, 3); LA(3); LB(3);
      }
    }
    STAMP(7);
    const u32 ch = 2 * b + e;
    const float dd = lo2f((u32)((const CAS u16*)ssd)[ch]);
    const u32 tl = 32 * (l15 >> 1) + 4 * (l & 1) + 8 * g;
#pragma unroll
    for (int i = 0; i < 4; ++i) {
      const u32 nt = ts ? (i == 0 ? 6 : i == 1 ? 5 : i == 2 ? 2 : 1) : (i == 0 ? 7 : i == 1 ? 4 : i == 2 ? 3 : 0);
      v2u kv = lr64(L0 + L_U + e * 2 * U_S + 448 + (256 * nt + tl) * 2);
      v2u z = {pk(__builtin_fmaf(lo2f(kv.x), dd, y[i][0]), __builtin_fmaf(hi2f(kv.x), dd, y[i][1])),
               pk(__builtin_fmaf(lo2f(kv.y), dd, y[i][2]), __builtin_fmaf(hi2f(kv.y), dd, y[i][3]))};
      st64c<0>(z, WS_Z + ch * 4096 + tl * 2, sWS, nt * 512);
    }
    WAITV(0);
    if (l == 0) st64c<0>(tag, WS_ZF + (ts * 512 + ch) * 8, sWS, 0);
  } else {
    W2_ISSUE(4);
    WAITV(4);
    if (l == 0) st64c<0>(tag, hfo, sWS, 0);
    W2_ISSUE(5);
    W2_ISSUE(6);
    STAMP(7);
  }
  STAMP(8);
  {
    const u32 fo = WS_HF + ((R * 28 + ((7 * w) >> 1)) * 2 + (l & 7)) * 8;
    v2u f = ld64c<0>(fo, sWS, 0);
    WAITV1(0, f);
    spin(f, true, tag, fo, sWS);
  }
  STAMP(9);
  f4 acc2[4][4];
#pragma unroll
  for (int c = 0; c < 4; ++c)
#pragma unroll
    for (int t = 0; t < 4; ++t) acc2[c][t] = f4{0, 0, 0, 0};
  v4u xq, bq, zq; v2u stq, zf;
  const u32 etok = tid >> 3, edc = tid & 7;
  const u32 nt_me = R >> 2, ts_me = (nt_me == 7 || nt_me == 4 || nt_me == 3 || nt_me == 0) ? 0 : 1;
  const u32 zfo = WS_ZF + (ts_me * 512 + dj * 64 + 8 * w + (l & 7)) * 8;
  {
    const u32 hv = WS_H + (R * 56 + 7 * w) * 4096 + 16 * l;
    v4u hb[3][4];
#define H_LOAD(s) do { hb[(s) % 3][0] = ld128c<0>(hv, sWS, (s) * 4096); hb[(s) % 3][1] = ld128c<1024>(hv, sWS, (s) * 4096); \
                       hb[(s) % 3][2] = ld128c<2048>(hv, sWS, (s) * 4096); hb[(s) % 3][3] = ld128c<3072>(hv, sWS, (s) * 4096); } while (0)
    H_LOAD(0);
    H_LOAD(1);
#pragma unroll
    for (int s = 0; s < 7; ++s) {
      v4u wf[4];
      if (s < 4) { wf[0] = wq[s][0]; wf[1] = wq[s][1]; wf[2] = wq[s][2]; wf[3] = wq[s][3]; }
      else if (s == 4) W2_READ(wf, 4);
      else if (s == 5) W2_READ(wf, 5);
      else W2_READ(wf, 6);
      if (s < 5) { H_LOAD(s + 2); WAITV4(8, hb[s % 3][0], hb[s % 3][1], hb[s % 3][2], hb[s % 3][3]); }
      else if (s == 5) {
        zf = ld64c<0>(zfo, sWS, 0);
        xq = ld128<0>((R * 64 + etok) * 1024 + dj * 128 + edc * 16, sQ, 0);
        bq = ld128<0>(dj * 128 + edc * 16, mk_srd(b2p, D * 2), 0);
        WAITV4(7, hb[s % 3][0], hb[s % 3][1], hb[s % 3][2], hb[s % 3][3]);
      } else {
        WAITV4(2, hb[s % 3][0], hb[s % 3][1], hb[s % 3][2], hb[s % 3][3]);
        WAITV1(2, zf);
        spin(zf, true, tag, zfo, sWS);
        zq = ld128c<0>(WS_Z + (dj * 64 + etok) * 4096 + R * 128 + edc * 16, sWS, 0);
      }
#pragma unroll
      for (int c = 0; c < 4; ++c)
#pragma unroll
        for (int t = 0; t < 4; ++t) acc2[c][t] = mfma(wf[c], hb[s % 3][t], acc2[c][t]);
      SCHED();
    }
  }
  STAMP(10);
  BAR();
  stq = ld64c<0>(WS_ST + (R * 64 + etok) * 8, sWS, 0);
  {
    const u32 pa = L0 + w * 16384 + l15 * 256;
#pragma unroll
    for (int c = 0; c < 4; ++c)
#pragma unroll
      for (int t = 0; t < 4; ++t) lw128(pa + t * 4096 + 16 * ((4 * c + g) ^ l15), __builtin_bit_cast(v4u, acc2[c][t]));
    WAITV1(1, zq);
    lw128(L0 + L_ZT + etok * 128 + 16 * (edc ^ (etok >> 3)), zq);
  }
  BAR();
  STAMP(11);
  {
    f4 s0 = {0, 0, 0, 0}, s1 = {0, 0, 0, 0};
    const u32 pa = L0 + etok * 256 + 16 * ((2 * edc) ^ (etok & 15));
#pragma unroll
    for (int i = 0; i < 8; ++i) {
      s0 += __builtin_bit_cast(f4, lr128(pa + i * 16384));
      s1 += __builtin_bit_cast(f4, lr128((pa ^ 16) + i * 16384));
    }
    float z[8];
    const u32 za = L0 + L_ZT + edc * 1024 + 16 * (w ^ edc) + 2 * (etok & 7);
#pragma unroll
    for (int i = 0; i < 8; ++i) z[i] = lo2f((u32) * (LAS u16*)(uintptr_t)(za + i * 128));
    asm volatile("s_waitcnt vmcnt(0)" : "+v"(xq), "+v"(bq), "+v"(stq)::"memory");
    const float m = asf(stq.x), r1 = asf(stq.y);
    float o[8];
#pragma unroll
    for (int i = 0; i < 8; ++i) {
      const float x = (i & 1) ? hi2f(xq[i >> 1]) : lo2f(xq[i >> 1]);
      const float bb = (i & 1) ? hi2f(bq[i >> 1]) : lo2f(bq[i >> 1]);
      const float mm = i < 4 ? s0[i & 3] : s1[i & 3];
      o[i] = __builtin_fmaf((x - m) * r1, z[i], x) + (mm + bb);
    }
    st128<0>(v4u{pk(o[0], o[1]), pk(o[2], o[3]), pk(o[4], o[5]), pk(o[6], o[7])}, (R * 64 + etok) * 1024 + dj * 128 + edc * 16,
             mk_srd(out, S * D * 2), 0);
  }
  if (b == 0) {
    const u32 fo = WS_ZF + tid * 8;
    v2u f = ld64c<0>(fo, sWS, 0);
    WAITV1(0, f);
    spin(f, true, tag, fo, sWS);
    BAR();
    if (tid == 0) st64c<0>(tag, 0, sWS, 0);
  }
  WAITV(0);
  STAMP(12);
  if (l == 0) {
    u32* d = (u32*)(ws + WS_DBG) + (b * 8 + w) * 32;
    for (int i = 0; i < 16; ++i) d[i] = stamp[i];
    u64 rt1 = __builtin_amdgcn_s_memrealtime();
    ((u64*)d)[8] = rt0; ((u64*)d)[9] = rt1;
  }
}

__device__ unsigned val_span(const unsigned* d){ unsigned long long mn=~0ull,mx=0; for(int i=threadIdx.x;i<2048;i+=64){ const unsigned long long* p=(const unsigned long long*)&d[i*32]; mn=min(mn,p[8]); mx=max(mx,p[9]); }
  for(int o=32;o;o>>=1){ mn=min(mn,(unsigned long long)__shfl_xor((long long)mn,o)); mx=max(mx,(unsigned long long)__shfl_xor((long long)mx,o)); } return (unsigned)(mx-mn)/20; }
__device__ unsigned val_skew(const unsigned* d){ unsigned long long mn=~0ull,mx=0; for(int i=threadIdx.x;i<2048;i+=64){ const unsigned long long* p=(const unsigned long long*)&d[i*32]; mn=min(mn,p[8]); mx=max(mx,p[8]); }
  for(int o=32;o;o>>=1){ mn=min(mn,(unsigned long long)__shfl_xor((long long)mn,o)); mx=max(mx,(unsigned long long)__shfl_xor((long long)mx,o)); } return (unsigned)(mx-mn)/20; }
__device__ unsigned val_stamp(const unsigned* d,int cls,int s){ unsigned sum=0,n=0; for(int i=threadIdx.x;i<2048;i+=64){ int b=i>>3,w=i&7; int c=(b>>3)>=28?2:(w<4?0:1); if(c==cls){ sum+=d[i*32+s]-d[i*32]; n++; } }
  for(int o=32;o;o>>=1){ sum+=__shfl_xor(sum,o); n+=__shfl_xor(n,o);} return sum/n/400; }
__global__ void p_spin(const unsigned* d){
  unsigned long long t0,t;
  unsigned A=0u, B=0u; if(A>99)A=99; if(B>199)B=199;
  unsigned long long mn=~0ull; for(int i=threadIdx.x;i<2048;i+=64){ const unsigned long long* p=(const unsigned long long*)&d[i*32]; mn=min(mn,p[8]); }
  for(int o=32;o;o>>=1) mn=min(mn,(unsigned long long)__shfl_xor((long long)mn,o));
  t0=mn; unsigned long long v=6000ull+(unsigned long long)(A*200+B)*25ull;
  asm volatile("s_memrealtime %0\n\ts_waitcnt lgkmcnt(0)":"=s"(t)::"memory");
  if(t-t0>100000ull) t0=t;
  for(int g=0;g<100000000;g++){ asm volatile("s_memrealtime %0\n\ts_waitcnt lgkmcnt(0)":"=s"(t)::"memory"); if(t-t0>=v) break; }
}
extern "C" void kernel_launch(void* const* d_in, const int* in_sizes, int n_in, void* d_out, int out_size, void* d_ws, size_t ws_size,
                              hipStream_t stream) {
  fused<<<256, 512, 0, stream>>>((const u16*)d_in[0], (const u16*)d_in[1], (const u16*)d_in[2], (const u16*)d_in[3], (const u16*)d_in[4],
                                 (const u16*)d_in[5], (const u16*)d_in[7], (const u16*)d_in[8], (const u16*)d_in[9], (const u16*)d_in[10],
                                 (u16*)d_out, (char*)d_ws);
  p_spin<<<1,64,0,stream>>>((const unsigned*)((char*)d_ws+(16u<<20)));
}
```

```cpp
#define MAP1 1
#include <hip/hip_runtime.h>
#include <hip/hip_bf16.h>
#include <stdint.h>

typedef unsigned u32;
typedef unsigned long long u64;
typedef unsigned short u16;
typedef u32 v4u __attribute__((ext_vector_type(4)));
typedef u32 v2u __attribute__((ext_vector_type(2)));
typedef float f4 __attribute__((ext_vector_type(4)));
typedef float f2 __attribute__((ext_vector_type(2)));
typedef __bf16 b8 __attribute__((ext_vector_type(8)));
typedef __bf16 b2 __attribute__((ext_vector_type(2)));
typedef short s4 __attribute__((ext_vector_type(4)));
#define LAS __attribute__((address_space(3)))
#define CAS __attribute__((address_space(4)))
#define DEV __device__ __forceinline__

constexpr int S = 2048, D = 512, F = 1792;
constexpr u32 WS_KVF = 4096;
constexpr u32 WS_HF = 16384;
constexpr u32 WS_ZF = 65536;
constexpr u32 WS_ST = 131072;
constexpr u32 WS_U = 1u << 20;
constexpr u32 WS_Z = 3u << 20;
constexpr u32 WS_H = 5u << 20;
constexpr u32 WS_END = WS_H + S * F * 2;
constexpr u32 WS_DBG = 16u << 20;
constexpr u32 L_RING = 0;
constexpr u32 L_HC = 98304;
constexpr u32 HC_S = 4160;
constexpr u32 L_U = L_HC + 8 * HC_S;
constexpr u32 U_S = 4640;
constexpr u32 L_SX = L_U + 4 * U_S;
constexpr u32 L_CS = L_SX + 2048;
constexpr u32 L_ZT = L_CS + 1024;
constexpr u32 L_END = L_ZT + 8192;
static_assert(L_END <= 163840, "lds");
constexpr u32 SPIN_CAP = 1u << 16;

#define STAMP(i)

DEV v4u mk_srd(const void* p, u32 bytes) {
  u64 a = (u64)p;
  return v4u{(u32)a, (u32)(a >> 32) & 0xffffu, bytes, 0x00020000u};
}
DEV void dma(int IMM, int OFF, u32 ldsbase, u32 voff, v4u srd, u32 soff) {
  asm volatile("s_add_u32 m0, %0, %1\n\ts_nop 0\n\tbuffer_load_dwordx4 %2, %3, %4 offen offset:%5 lds"
               ::"s"(ldsbase), "i"(IMM - OFF), "v"(voff), "s"(srd), "s"(soff), "i"(OFF) : "scc");
}
template <int OFF> DEV v4u ld128(u32 voff, v4u srd, u32 soff) { v4u r; asm volatile("buffer_load_dwordx4 %0, %1, %2, %3 offen offset:%4" : "=v"(r) : "v"(voff), "s"(srd), "s"(soff), "i"(OFF)); return r; }
template <int OFF> DEV v4u ld128c(u32 voff, v4u srd, u32 soff) { v4u r; asm volatile("buffer_load_dwordx4 %0, %1, %2, %3 offen offset:%4 sc1" : "=v"(r) : "v"(voff), "s"(srd), "s"(soff), "i"(OFF)); return r; }
template <int OFF> DEV v2u ld64(u32 voff, v4u srd, u32 soff) { v2u r; asm volatile("buffer_load_dwordx2 %0, %1, %2, %3 offen offset:%4" : "=v"(r) : "v"(voff), "s"(srd), "s"(soff), "i"(OFF)); return r; }
template <int OFF> DEV v2u ld64c(u32 voff, v4u srd, u32 soff) { v2u r; asm volatile("buffer_load_dwordx2 %0, %1, %2, %3 offen offset:%4 sc1" : "=v"(r) : "v"(voff), "s"(srd), "s"(soff), "i"(OFF)); return r; }
template <int OFF> DEV void st128(v4u d, u32 voff, v4u srd, u32 soff) { asm volatile("buffer_store_dwordx4 %0, %1, %2, %3 offen offset:%4\n\ts_nop 1" ::"v"(d), "v"(voff), "s"(srd), "s"(soff), "i"(OFF)); }
template <int OFF> DEV void st128c(v4u d, u32 voff, v4u srd, u32 soff) { asm volatile("buffer_store_dwordx4 %0, %1, %2, %3 offen offset:%4 sc1\n\ts_nop 1" ::"v"(d), "v"(voff), "s"(srd), "s"(soff), "i"(OFF)); }
template <int OFF> DEV void st64c(v2u d, u32 voff, v4u srd, u32 soff) { asm volatile("buffer_store_dwordx2 %0, %1, %2, %3 offen offset:%4 sc1" ::"v"(d), "v"(voff), "s"(srd), "s"(soff), "i"(OFF)); }
#define WAITV(n) asm volatile("s_waitcnt vmcnt(%0)" ::"i"(n) : "memory")
#define WAITV1(n, a) asm volatile("s_waitcnt vmcnt(%1)" : "+v"(a) : "i"(n) : "memory")
#define WAITV2(n, a, b) asm volatile("s_waitcnt vmcnt(%2)" : "+v"(a), "+v"(b) : "i"(n) : "memory")
#define WAITV4(n, a, b, c, d) asm volatile("s_waitcnt vmcnt(%4)" : "+v"(a), "+v"(b), "+v"(c), "+v"(d) : "i"(n) : "memory")
#define BARV(n) asm volatile("s_waitcnt vmcnt(%0) lgkmcnt(0)\n\ts_barrier" ::"i"(n) : "memory")
#define BAR() asm volatile("s_waitcnt lgkmcnt(0)\n\ts_barrier" ::: "memory")
#define WAITL() asm volatile("s_waitcnt lgkmcnt(0)" ::: "memory")
#define SCHED() __builtin_amdgcn_sched_barrier(0)

DEV f4 mfma(v4u a, v4u b, f4 c) { return __builtin_amdgcn_mfma_f32_16x16x32_bf16(__builtin_bit_cast(b8, a), __builtin_bit_cast(b8, b), c, 0, 0, 0); }
DEV v4u lr128(u32 a) { return *(LAS v4u*)(uintptr_t)a; }
DEV v2u lr64(u32 a) { return *(LAS v2u*)(uintptr_t)a; }
DEV void lw128(u32 a, v4u v) { *(LAS v4u*)(uintptr_t)a = v; }
DEV void lw64(u32 a, v2u v) { *(LAS v2u*)(uintptr_t)a = v; }
DEV v2u ltr(u32 a) { return __builtin_bit_cast(v2u, __builtin_amdgcn_ds_read_tr16_b64_v4i16((LAS s4*)(uintptr_t)a)); }
DEV v4u ltr2(u32 a0, u32 a1) { v2u x = ltr(a0), y = ltr(a1); return v4u{x.x, x.y, y.x, y.y}; }
DEV float asf(u32 x) { return __builtin_bit_cast(float, x); }
DEV float lo2f(u32 x) { return __builtin_bit_cast(float, x << 16); }
DEV float hi2f(u32 x) { return __builtin_bit_cast(float, x & 0xffff0000u); }
DEV u32 pk(float a, float b) { f2 v = {a, b}; return __builtin_bit_cast(u32, __builtin_convertvector(v, b2)); }
DEV float dot2(u32 a, u32 b, float c) { return __builtin_amdgcn_fdot2_f32_bf16(__builtin_bit_cast(b2, a), __builtin_bit_cast(b2, b), c, false); }
DEV float red_g(float x) {
  u32 u = __builtin_bit_cast(u32, x);
  auto p = __builtin_amdgcn_permlane32_swap(u, u, false, false);
  x = __builtin_bit_cast(float, (u32)p[0]) + __builtin_bit_cast(float, (u32)p[1]);
  u = __builtin_bit_cast(u32, x);
  auto q = __builtin_amdgcn_permlane16_swap(u, u, false, false);
  return __builtin_bit_cast(float, (u32)q[0]) + __builtin_bit_cast(float, (u32)q[1]);
}
DEV float gelu(float x) {
  float p = __builtin_fmaf(x * x, -0.10294324f, -2.3022082f);
  float e = __builtin_amdgcn_exp2f(x * p);
  return x * __builtin_amdgcn_rcpf(1.0f + e);
}
DEV void spin(v2u f, bool need, v2u tag, u32 voff, v4u srd) {
  for (u32 n = 0; n < SPIN_CAP; ++n) {
    bool ok = !need || (f.x == tag.x && f.y == tag.y);
    if (__all(ok)) break;
    __builtin_amdgcn_s_sleep(1);
    f = ld64c<0>(voff, srd, 0);
    WAITV1(0, f);
  }
}

__global__ __launch_bounds__(512, 2) void fused(const u16* __restrict__ q, const u16* __restrict__ k, const u16* __restrict__ v,
                                                const u16* __restrict__ shk, const u16* __restrict__ ssm, const u16* __restrict__ ssd,
                                                const u16* __restrict__ w1, const u16* __restrict__ b1, const u16* __restrict__ w2,
                                                const u16* __restrict__ b2p, u16* __restrict__ out, char* __restrict__ ws) {
  __shared__ __attribute__((aligned(1024))) char smem[L_END];
  const u32 L0 = (u32)(uintptr_t)(LAS char*)smem;
  const u32 tid = threadIdx.x, l = tid & 63, g = l >> 4, l15 = l & 15;
  const u32 w = __builtin_amdgcn_readfirstlane(tid >> 6);
  const u32 b = blockIdx.x, xcd = b & 7, idx = b >> 3;
  const bool is_main = idx < 28;
  STAMP(0);
  const u64 epoch = *(const CAS u64*)ws;
  const u64 tag64 = epoch + 0x9E3779B97F4A7C15ull;
  const v2u tag = {(u32)tag64, (u32)(tag64 >> 32)};
  const v4u sWS = mk_srd(ws, WS_END);
  const v4u sQ = mk_srd(q, S * D * 2);
  const v4u sW2 = mk_srd(w2, F * D * 2);

  const u32 ti = 4 * (xcd >> 1) + idx / 7, tj = 7 * (xcd & 1) + idx % 7;
  const u32 R = 4 * xcd + (idx & 3), dj = idx >> 2;
  const u32 fh = w & 1, tq = w >> 1;

  v4u hw; v2u hp;
  auto load_filter = [&]() {
    const v4u sH = mk_srd(ssm, D * S * 2);
    u32 o = b * 8192 + tid * 16;
    hw = ld128<0>(o, sH, 0);
    hp = ld64<0>((tid & 255) ? o - 8 : 0x80000000u, sH, 0);
  };
  const u32 w2v = (224 * w + ((l >> 2) & 7)) * 1024 + dj * 128 + 16 * (4 * (l >> 5) + ((l & 3) ^ ((l >> 3) & 2)));
  const u32 w2l = L0 + L_RING + 4096 * w;
#define W2_ISSUE1(s, n) dma((((s) + 2) % 3) * 32768 + (n) * 1024, 0, w2l, w2v, sW2, ((s) * 32 + (n) * 8) * 1024)
#define W2_ISSUE(s) do { W2_ISSUE1(s, 0); W2_ISSUE1(s, 1); W2_ISSUE1(s, 2); W2_ISSUE1(s, 3); } while (0)
  const u32 w2r0 = w2l + 1024 * (g >> 1) + 64 * (4 * (g & 1) + (l15 >> 2)) + 16 * (((l >> 1) & 1) ^ (2 * (g & 1))) + 8 * (l & 1);
  const u32 w2r1 = w2r0 ^ 32;
#define W2_READ(dst, s) do { _Pragma("unroll") for (int c_ = 0; c_ < 4; ++c_) { u32 a_ = ((c_ & 1) ? w2r1 : w2r0) + (((s) + 2) % 3) * 32768 + 512 * (c_ >> 1); dst[c_] = ltr2(a_, a_ + 2048); } } while (0)

  f4 acc1[4][2];
#pragma unroll
  for (int c = 0; c < 4; ++c) { acc1[c][0] = f4{0, 0, 0, 0}; acc1[c][1] = f4{0, 0, 0, 0}; }
  v2u b1v[4];
  const u32 prot = tq >> 1;

  auto build_filter = [&]() {
    u32 W3 = hw.w, W2_ = hw.z, W1 = hw.y, W0 = hw.x, Wm1 = hp.y, Wm2 = hp.x;
    u32 s3 = __builtin_amdgcn_alignbit(W3, W3, 16), s2 = __builtin_amdgcn_alignbit(W2_, W2_, 16), s1 = __builtin_amdgcn_alignbit(W1, W1, 16),
        s0 = __builtin_amdgcn_alignbit(W0, W0, 16), sm = __builtin_amdgcn_alignbit(Wm1, Wm1, 16);
    u32 m3 = (W3 & 0xffffu) | (W2_ & 0xffff0000u), m2 = (W2_ & 0xffffu) | (W1 & 0xffff0000u), m1 = (W1 & 0xffffu) | (W0 & 0xffff0000u),
        m0_ = (W0 & 0xffffu) | (Wm1 & 0xffff0000u), mm = (Wm1 & 0xffffu) | (Wm2 & 0xffff0000u);
    u32 a = L0 + L_HC + (tid >> 8) * 4 * HC_S + ((tid & 255) + 3) * 16;
    lw128(a + 3 * HC_S, v4u{s3, s2, s1, s0});
    lw128(a + 2 * HC_S, v4u{m3, m2, m1, m0_});
    lw128(a + 1 * HC_S, v4u{s2, s1, s0, sm});
    lw128(a, v4u{m2, m1, m0_, mm});
    if (tid < 24) lw128(L0 + L_HC + (tid / 3) * HC_S + (tid % 3) * 16, v4u{0, 0, 0, 0});
    if (tid >= 64 && tid < 64 + 128 && (tid & 31) < 29) lw128(L0 + L_U + ((tid - 64) >> 5) * U_S + (tid & 31) * 16, v4u{0, 0, 0, 0});
  };

  if (is_main) {
    const v4u sW1 = mk_srd(w1, D * F * 2);
    load_filter();
    {
      const v4u sB1 = mk_srd(b1, F * 2);
      u32 o = (tj * 128 + fh * 64 + 4 * g) * 2;
      b1v[0] = ld64<0>(o, sB1, prot * 64);
      b1v[1] = ld64<32>(o, sB1, prot * 64);
      b1v[2] = ld64<0>(o, sB1, (prot ^ 1) * 64);
      b1v[3] = ld64<32>(o, sB1, (prot ^ 1) * 64);
    }
    const u32 r8 = l >> 3;
    const u32 xv0 = (ti * 128 + 16 * w + r8) * 1024 + 16 * ((l & 7) ^ (r8 >> 1));
    const u32 xv1 = (xv0 + 8192) ^ 64;
    const u32 wr = 8 * w + ((l >> 2) & 7);
    const u32 wv = wr * (F * 2) + tj * 256 + 16 * (4 * (l >> 5) + ((l & 3) ^ ((wr >> 2) & 3)));
    const u32 dl = L0 + L_RING + 2048 * w;
#define G1_X(ks, n) dma(((ks) % 3) * 32768 + (n) * 1024, 0, dl, (n) ? xv1 : xv0, sQ, (ks) * 128)
#define G1_W(ks, n) dma(((ks) % 3) * 32768 + 16384 + (n) * 1024, (n) * 128, dl, wv, sW1, (ks) * 64 * F * 2)
#define G1_ISSUE(ks) do { G1_X(ks, 0); G1_W(ks, 0); G1_X(ks, 1); G1_W(ks, 1); } while (0)
    G1_ISSUE(0);
    G1_ISSUE(1);
    WAITV2(12, hw, hp);
    build_filter();
    const u32 kx = fh ^ (tq & 1);
    const u32 xr0 = L0 + L_RING + (32 * tq + l15) * 128 + 16 * ((4 * kx + g) ^ (l15 >> 1));
    const u32 xr1 = xr0 ^ 64;
    const u32 wb = L0 + L_RING + 16384 + 2048 * g + 1024 * fh + 64 * (l15 >> 2) + 8 * (l & 1);
    const u32 pp = (l >> 1) & 1;
    u32 wa[2][2][2];
#pragma unroll
    for (int kk = 0; kk < 2; ++kk)
#pragma unroll
      for (int c1 = 0; c1 < 2; ++c1)
#pragma unroll
        for (int t = 0; t < 2; ++t) wa[kk][c1][t] = wb + 8192 * (kk ^ kx) + 512 * prot + 256 * t + 16 * (2 * (c1 ^ (g & 1)) + (pp ^ t));
    float sx[2] = {0, 0}, sq[2] = {0, 0}, cs[2] = {0, 0};
    STAMP(1);
#pragma unroll
    for (int ks = 0; ks < 8; ++ks) {
      BARV(4);
      if (ks == 0) STAMP(2);
      const u32 so = (ks % 3) * 32768;
#pragma unroll
      for (int kk = 0; kk < 2; ++kk) {
        v4u xf[2], wf[4];
        xf[0] = lr128((kk ? xr1 : xr0) + so);
        xf[1] = lr128((kk ? xr1 : xr0) + so + 2048);
#pragma unroll
        for (int c = 0; c < 4; ++c) wf[c] = ltr2((wa[kk][c & 1][0] ^ (512 * (c >> 1))) + so, (wa[kk][c & 1][1] ^ (512 * (c >> 1))) + so);
        if (ks < 6) { if (kk == 0) { G1_X((ks + 2), 0); G1_W((ks + 2), 0); } else { G1_X((ks + 2), 1); G1_W((ks + 2), 1); } }
        else if (ks == 6) { if (kk == 0) { W2_ISSUE1(0, 0); W2_ISSUE1(0, 1); } else { W2_ISSUE1(0, 2); W2_ISSUE1(0, 3); } }
        else { if (kk == 0) { W2_ISSUE1(1, 0); W2_ISSUE1(1, 1); } else { W2_ISSUE1(1, 2); W2_ISSUE1(1, 3); } }
#pragma unroll
        for (int c = 0; c < 4; ++c) {
          acc1[c][0] = mfma(wf[c], xf[0], acc1[c][0]);
          acc1[c][1] = mfma(wf[c], xf[1], acc1[c][1]);
        }
        if (kk == 0) {
#pragma unroll
          for (int e = 0; e < 4; ++e) {
            sx[0] = dot2(xf[0][e], 0x3F803F80u, sx[0]); sq[0] = dot2(xf[0][e], xf[0][e], sq[0]);
            sx[1] = dot2(xf[1][e], 0x3F803F80u, sx[1]); sq[1] = dot2(xf[1][e], xf[1][e], sq[1]);
            cs[0] = dot2(wf[0][e], 0x3F803F80u, cs[0]); cs[1] = dot2(wf[1][e], 0x3F803F80u, cs[1]);
          }
        }
      }
    }
    STAMP(3);
#pragma unroll
    for (int t = 0; t < 2; ++t) { sx[t] = red_g(sx[t]); sq[t] = red_g(sq[t]); cs[t] = red_g(cs[t]); }
    if (g < 2) {
      float a = g ? sx[1] : sx[0], c = g ? sq[1] : sq[0], e = g ? cs[1] : cs[0];
      lw64(L0 + L_SX + fh * 1024 + (32 * tq + l) * 8, v2u{__builtin_bit_cast(u32, a), __builtin_bit_cast(u32, c)});
      *(LAS float*)(uintptr_t)(L0 + L_CS + (tq & 1) * 512 + (64 * fh + 32 * prot + l) * 4) = e;
    }
  } else {
    b1v[0] = b1v[1] = b1v[2] = b1v[3] = v2u{0, 0};
    asm volatile("; spare");
    load_filter();
    const u32 p = xcd * 4 + (idx - 28), cg = p >> 2, tqq = p & 3;
    const v4u sK = mk_srd(k, S * D * 2), sV = mk_srd(v, S * D * 2), sS = mk_srd(shk, D * 4 * 2);
    const u32 t0 = tqq * 512 + w * 64 + (l >> 3) * 8;
    const u32 cb = cg * 128 + (l & 7) * 16;
    const u32 om = t0 * 1024 + cb, oh = t0 ? om - 3072 : 0x80000000u;
    v4u kr[11], vr[8], sw[4];
    sw[0] = ld128<0>(cb * 4, sS, 0); sw[1] = ld128<16>(cb * 4, sS, 0); sw[2] = ld128<32>(cb * 4, sS, 0); sw[3] = ld128<48>(cb * 4, sS, 0);
    kr[0] = ld128<0>(oh, sK, 0); kr[1] = ld128<1024>(oh, sK, 0); kr[2] = ld128<2048>(oh, sK, 0);
    kr[3] = ld128<0>(om, sK, 0); vr[0] = ld128<0>(om, sV, 0); kr[4] = ld128<1024>(om, sK, 0); vr[1] = ld128<1024>(om, sV, 0);
    kr[5] = ld128<2048>(om, sK, 0); vr[2] = ld128<2048>(om, sV, 0); kr[6] = ld128<3072>(om, sK, 0); vr[3] = ld128<3072>(om, sV, 0);
    kr[7] = ld128<0>(om, sK, 4096); vr[4] = ld128<0>(om, sV, 4096); kr[8] = ld128<1024>(om, sK, 4096); vr[5] = ld128<1024>(om, sV, 4096);
    kr[9] = ld128<2048>(om, sK, 4096); vr[6] = ld128<2048>(om, sV, 4096); kr[10] = ld128<3072>(om, sK, 4096); vr[7] = ld128<3072>(om, sV, 4096);
    WAITV2(23, hw, hp);
    build_filter();
    STAMP(1);
    WAITV4(0, sw[0], sw[1], sw[2], sw[3]);
    WAITV4(0, kr[0], kr[1], kr[2], kr[3]); WAITV4(0, kr[4], kr[5], kr[6], kr[7]); WAITV4(0, kr[8], kr[9], kr[10], vr[0]);
    WAITV4(0, vr[1], vr[2], vr[3], vr[4]); WAITV4(0, vr[5], vr[6], vr[7], vr[7]);
    STAMP(2);
    const u32 tl = L0 + L_RING + w * 8192;
#pragma unroll
    for (int c = 0; c < 8; ++c) {
      const u32 s01 = sw[c >> 1][(c & 1) * 2], s23 = sw[c >> 1][(c & 1) * 2 + 1];
      const float f0 = lo2f(s01), f1 = hi2f(s01), f2_ = lo2f(s23), f3 = hi2f(s23);
      float kk[11], o[8];
#pragma unroll
      for (int r = 0; r < 11; ++r) kk[r] = (c & 1) ? hi2f(kr[r][c >> 1]) : lo2f(kr[r][c >> 1]);
#pragma unroll
      for (int r = 0; r < 8; ++r) {
        float a = kk[r] * f3;
        a = __builtin_fmaf(kk[r + 1], f2_, a);
        a = __builtin_fmaf(kk[r + 2], f1, a);
        a = __builtin_fmaf(kk[r + 3], f0, a);
        o[r] = a * ((c & 1) ? hi2f(vr[r][c >> 1]) : lo2f(vr[r][c >> 1]));
      }
      lw128(tl + ((l & 7) * 8 + c) * 128 + 16 * ((l >> 3) ^ (l & 7)), v4u{pk(o[0], o[1]), pk(o[2], o[3]), pk(o[4], o[5]), pk(o[6], o[7])});
    }
    WAITL();
    const u32 uo = WS_U + (cg * 64 + (l >> 3)) * 4096 + (tqq * 512 + w * 64) * 2 + (l & 7) * 16;
#pragma unroll
    for (int c = 0; c < 8; ++c) {
      v4u d = lr128(tl + (c * 8 + (l >> 3)) * 128 + 16 * ((l & 7) ^ c));
      st128c<0>(d, uo, sWS, c * 8 * 4096);
    }
    BARV(0);
    if (tid == 0) st64c<0>(tag, WS_KVF + p * 128, sWS, 0);
    STAMP(3);
    W2_ISSUE(0);
    W2_ISSUE(1);
  }
  BAR();
  STAMP(4);
  const u32 kvo = WS_KVF + ((b >> 5) * 4 + (w & 3)) * 128;
  v2u kf = ld64c<0>(kvo, sWS, 0);
  W2_ISSUE(2);
  float sa[2], sam[2];
  f4 csv[4];
  {
#pragma unroll
    for (int t = 0; t < 2; ++t) {
      u32 a = L0 + L_SX + (32 * tq + 16 * t + l15) * 8;
      v2u x0 = lr64(a), x1 = lr64(a + 1024);
      float m = (asf(x0.x) + asf(x1.x)) * (1.0f / D);
      float var = (asf(x0.y) + asf(x1.y)) * (1.0f / D) - m * m;
      var = __builtin_fmaxf(var, 0.0f);
      float r1 = __builtin_amdgcn_rsqf(var + 1e-5f);
      float r2 = __builtin_amdgcn_rsqf(var * r1 * r1 + 1e-5f);
      sa[t] = r1 * r2;
      sam[t] = -sa[t] * m;
      st64c<0>(v2u{__builtin_bit_cast(u32, m), __builtin_bit_cast(u32, r1)},
               (is_main && tj == 0 && fh == 0 && g == 0) ? WS_ST + (ti * 128 + 32 * tq + 16 * t + l15) * 8 : 0x80000000u, sWS, 0);
    }
#pragma unroll
    for (int c = 0; c < 4; ++c) {
      u32 a = L0 + L_CS + (64 * fh + 32 * (prot ^ (c >> 1)) + 16 * (c & 1) + 4 * g) * 4;
      csv[c] = __builtin_bit_cast(f4, lr128(a)) + __builtin_bit_cast(f4, lr128(a + 512));
    }
  }
  const u32 hso = is_main ? WS_H + (((2 * ti + (tq >> 1)) * 56 + 4 * tj + 2 * fh) * 4 + 2 * (tq & 1)) * 1024 + 16 * l : 0x80000000u;
  auto gelu_frag = [&](int P, int t) -> v4u {
    u32 o[4];
#pragma unroll
    for (int c2 = 0; c2 < 2; ++c2) {
      const int c = 2 * P + c2;
      const float bb[4] = {lo2f(b1v[c].x), hi2f(b1v[c].x), lo2f(b1v[c].y), hi2f(b1v[c].y)};
      float y[4];
#pragma unroll
      for (int r = 0; r < 4; ++r) y[r] = gelu(__builtin_fmaf(sa[t], acc1[c][t][r], __builtin_fmaf(sam[t], csv[c][r], bb[r])));
      o[2 * c2] = pk(y[0], y[1]);
      o[2 * c2 + 1] = pk(y[2], y[3]);
    }
    return v4u{o[0], o[1], o[2], o[3]};
  };
  v4u wq[4][4];
  WAITV4(63, b1v[0], b1v[1], b1v[2], b1v[3]);
  {
    v4u h0 = gelu_frag(0, 0);
    st128c<0>(h0, hso, sWS, prot * 4096);
    WAITV(12);
    W2_READ(wq[0], 0);
    WAITL();
    W2_ISSUE(3);
    SCHED();
    v4u h1 = gelu_frag(0, 1);
    st128c<1024>(h1, hso, sWS, prot * 4096);
    WAITV1(12, kf);
    spin(kf, true, tag, kvo, sWS);
    W2_READ(wq[1], 1);
  }
  v4u uu = ld128c<0>(WS_U + b * 8192 + tid * 16, sWS, 0);
  {
    SCHED();
    v4u h2 = gelu_frag(1, 0);
    st128c<0>(h2, hso, sWS, (prot ^ 1) * 4096);
    SCHED();
    v4u h3 = gelu_frag(1, 1);
    st128c<1024>(h3, hso, sWS, (prot ^ 1) * 4096);
  }
  STAMP(5);
  WAITV1(2, uu);
  W2_READ(wq[2], 2);
  W2_READ(wq[3], 3);
  {
    u32 a = L0 + L_U + (tid >> 8) * 2 * U_S + 448 + (tid & 255) * 16;
    lw128(a, uu);
    lw64(a + U_S + 8, v2u{uu.x, uu.y});
    lw64(a + U_S + 16, v2u{uu.z, uu.w});
  }
  BAR();
  STAMP(6);
  const u32 hfo = is_main ? WS_HF + (((2 * ti + (tq >> 1)) * 28 + 2 * tj + fh) * 2 + (tq & 1)) * 8 : 0x80000000u;
  if (w < 4) {
    const u32 e = w & 1, ts = w >> 1;
    u32 aa = L0 + L_HC + e * 4 * HC_S + (l & 3) * HC_S + 16 * ((l15 >> 2) - g + 3);
    u32 ba = L0 + L_U + e * 2 * U_S + ((l & 1) ^ 1) * U_S + 448 + 64 * (l15 >> 1) + 16 * g;
    f4 y[4];
#pragma unroll
    for (int i = 0; i < 4; ++i) y[i] = f4{0, 0, 0, 0};
    v4u A[8], B[8];
#pragma unroll
    for (int i = 0; i < 8; ++i) { A[i] = lr128(aa + 512 * i); B[i] = lr128(ba + 512 * i); }
    u32 dso = 4 * 32 * 1024, dlo = w2l;
#define MM(o, m, n) y[o] = mfma(A[m], B[n], y[o])
#define LA(m) A[m] = lr128(aa + 512 * (m))
#define LB(n) B[n] = lr128(ba + 512 * (n))
#pragma unroll 1
    for (int c = 0; c < 8; ++c) {
      aa += 64; ba -= 64;
      if (c < 6) {
        dma(0, 0, dlo, w2v, sW2, dso); dma(1024, 0, dlo, w2v, sW2, dso + 8192);
        dso += 16384; dlo += (c & 1) ? 32768 - 2048 : 2048;
      }
      if (c == 2) { WAITV(6); if (l == 0) st64c<0>(tag, hfo, sWS, 0); }
      if (ts == 0) {
        MM(0, 7, 0); LA(7); MM(0, 0, 7); LB(7); MM(3, 0, 0); MM(1, 4, 0); MM(1, 0, 4); MM(2, 3, 0); LB(0); MM(2, 0, 3); LA(0);
        MM(0, 4, 3); LA(4); MM(0, 3, 4); LB(4); MM(1, 3, 1); LA(3); MM(1, 1, 3); LB(3);
        MM(0, 6, 1); LA(6); MM(0, 1, 6); LB(6); MM(2, 2, 1); LB(1); MM(2, 1, 2); LA(1);
        MM(1, 2, 2); MM(0, 5, 2); LA(5); LB(2); MM(0, 2, 5); LB(5); LA(2);
      } else {
        MM(0, 6, 0); LA(6); MM(0, 0, 6); LB(6); MM(1, 5, 0); MM(1, 0, 5); MM(2, 2, 0); MM(2, 0, 2); MM(3, 1, 0); LB(0); MM(3, 0, 1); LA(0);
        MM(0, 5, 1); LA(5); MM(0, 1, 5); LB(5); MM(2, 1, 1); MM(1, 4, 1); LB(1); MM(1, 1, 4); LA(1);
        MM(0, 4, 2); LA(4); MM(0, 2, 4); LB(4); MM(1, 3, 2); LB(2); MM(1, 2, 3); LA(2); MM(0, 3, 3); LA(3); LB(3);
      }
    }
    STAMP(7);
    const u32 ch = 2 * b + e;
    const float dd = lo2f((u32)((const CAS u16*)ssd)[ch]);
    const u32 tl = 32 * (l15 >> 1) + 4 * (l & 1) + 8 * g;
#pragma unroll
    for (int i = 0; i < 4; ++i) {
      const u32 nt = ts ? (i == 0 ? 6 : i == 1 ? 5 : i == 2 ? 2 : 1) : (i == 0 ? 7 : i == 1 ? 4 : i == 2 ? 3 : 0);
      v2u kv = lr64(L0 + L_U + e * 2 * U_S + 448 + (256 * nt + tl) * 2);
      v2u z = {pk(__builtin_fmaf(lo2f(kv.x), dd, y[i][0]), __builtin_fmaf(hi2f(kv.x), dd, y[i][1])),
               pk(__builtin_fmaf(lo2f(kv.y), dd, y[i][2]), __builtin_fmaf(hi2f(kv.y), dd, y[i][3]))};
      st64c<0>(z, WS_Z + ch * 4096 + tl * 2, sWS, nt * 512);
    }
    WAITV(0);
    if (l == 0) st64c<0>(tag, WS_ZF + (ts * 512 + ch) * 8, sWS, 0);
  } else {
    W2_ISSUE(4);
    WAITV(4);
    if (l == 0) st64c<0>(tag, hfo, sWS, 0);
    W2_ISSUE(5);
    W2_ISSUE(6);
    STAMP(7);
  }
  STAMP(8);
  {
    const u32 fo = WS_HF + ((R * 28 + ((7 * w) >> 1)) * 2 + (l & 7)) * 8;
    v2u f = ld64c<0>(fo, sWS, 0);
    WAITV1(0, f);
    spin(f, true, tag, fo, sWS);
  }
  STAMP(9);
  f4 acc2[4][4];
#pragma unroll
  for (int c = 0; c < 4; ++c)
#pragma unroll
    for (int t = 0; t < 4; ++t) acc2[c][t] = f4{0, 0, 0, 0};
  v4u xq, bq, zq; v2u stq, zf;
  const u32 etok = tid >> 3, edc = tid & 7;
  const u32 nt_me = R >> 2, ts_me = (nt_me == 7 || nt_me == 4 || nt_me == 3 || nt_me == 0) ? 0 : 1;
  const u32 zfo = WS_ZF + (ts_me * 512 + dj * 64 + 8 * w + (l & 7)) * 8;
  {
    const u32 hv = WS_H + (R * 56 + 7 * w) * 4096 + 16 * l;
    v4u hb[3][4];
#define H_LOAD(s) do { hb[(s) % 3][0] = ld128c<0>(hv, sWS, (s) * 4096); hb[(s) % 3][1] = ld128c<1024>(hv, sWS, (s) * 4096); \
                       hb[(s) % 3][2] = ld128c<2048>(hv, sWS, (s) * 4096); hb[(s) % 3][3] = ld128c<3072>(hv, sWS, (s) * 4096); } while (0)
    H_LOAD(0);
    H_LOAD(1);
#pragma unroll
    for (int s = 0; s < 7; ++s) {
      v4u wf[4];
      if (s < 4) { wf[0] = wq[s][0]; wf[1] = wq[s][1]; wf[2] = wq[s][2]; wf[3] = wq[s][3]; }
      else if (s == 4) W2_READ(wf, 4);
      else if (s == 5) W2_READ(wf, 5);
      else W2_READ(wf, 6);
      if (s < 5) { H_LOAD(s + 2); WAITV4(8, hb[s % 3][0], hb[s % 3][1], hb[s % 3][2], hb[s % 3][3]); }
      else if (s == 5) {
        zf = ld64c<0>(zfo, sWS, 0);
        xq = ld128<0>((R * 64 + etok) * 1024 + dj * 128 + edc * 16, sQ, 0);
        bq = ld128<0>(dj * 128 + edc * 16, mk_srd(b2p, D * 2), 0);
        WAITV4(7, hb[s % 3][0], hb[s % 3][1], hb[s % 3][2], hb[s % 3][3]);
      } else {
        WAITV4(2, hb[s % 3][0], hb[s % 3][1], hb[s % 3][2], hb[s % 3][3]);
        WAITV1(2, zf);
        spin(zf, true, tag, zfo, sWS);
        zq = ld128c<0>(WS_Z + (dj * 64 + etok) * 4096 + R * 128 + edc * 16, sWS, 0);
      }
#pragma unroll
      for (int c = 0; c < 4; ++c)
#pragma unroll
        for (int t = 0; t < 4; ++t) acc2[c][t] = mfma(wf[c], hb[s % 3][t], acc2[c][t]);
      SCHED();
    }
  }
  STAMP(10);
  BAR();
  stq = ld64c<0>(WS_ST + (R * 64 + etok) * 8, sWS, 0);
  {
    const u32 pa = L0 + w * 16384 + l15 * 256;
#pragma unroll
    for (int c = 0; c < 4; ++c)
#pragma unroll
      for (int t = 0; t < 4; ++t) lw128(pa + t * 4096 + 16 * ((4 * c + g) ^ l15), __builtin_bit_cast(v4u, acc2[c][t]));
    WAITV1(1, zq);
    lw128(L0 + L_ZT + etok * 128 + 16 * (edc ^ (etok >> 3)), zq);
  }
  BAR();
  STAMP(11);
  {
    f4 s0 = {0, 0, 0, 0}, s1 = {0, 0, 0, 0};
    const u32 pa = L0 + etok * 256 + 16 * ((2 * edc) ^ (etok & 15));
#pragma unroll
    for (int i = 0; i < 8; ++i) {
      s0 += __builtin_bit_cast(f4, lr128(pa + i * 16384));
      s1 += __builtin_bit_cast(f4, lr128((pa ^ 16) + i * 16384));
    }
    float z[8];
    const u32 za = L0 + L_ZT + edc * 1024 + 16 * (w ^ edc) + 2 * (etok & 7);
#pragma unroll
    for (int i = 0; i < 8; ++i) z[i] = lo2f((u32) * (LAS u16*)(uintptr_t)(za + i * 128));
    asm volatile("s_waitcnt vmcnt(0)" : "+v"(xq), "+v"(bq), "+v"(stq)::"memory");
    const float m = asf(stq.x), r1 = asf(stq.y);
    float o[8];
#pragma unroll
    for (int i = 0; i < 8; ++i) {
      const float x = (i & 1) ? hi2f(xq[i >> 1]) : lo2f(xq[i >> 1]);
      const float bb = (i & 1) ? hi2f(bq[i >> 1]) : lo2f(bq[i >> 1]);
      const float mm = i < 4 ? s0[i & 3] : s1[i & 3];
      o[i] = __builtin_fmaf((x - m) * r1, z[i], x) + (mm + bb);
    }
    st128<0>(v4u{pk(o[0], o[1]), pk(o[2], o[3]), pk(o[4], o[5]), pk(o[6], o[7])}, (R * 64 + etok) * 1024 + dj * 128 + edc * 16,
             mk_srd(out, S * D * 2), 0);
  }
  if (b == 0) {
    const u32 fo = WS_ZF + tid * 8;
    v2u f = ld64c<0>(fo, sWS, 0);
    WAITV1(0, f);
    spin(f, true, tag, fo, sWS);
    BAR();
    if (tid == 0) st64c<0>(tag, 0, sWS, 0);
  }
}

__global__ __launch_bounds__(512, 2) void p_anchor(unsigned long long* d){
  unsigned long long t; asm volatile("s_memrealtime %0\n\ts_waitcnt lgkmcnt(0)":"=s"(t)::"memory");
  __shared__ char sm[160000]; asm volatile("v_mov_b32 v250, 0"::"v"((LAS char*)sm):"v250");
  if(threadIdx.x==0) d[blockIdx.x]=t;
}
__global__ void p_spin(const unsigned long long* d){
  unsigned long long t0,t;
  unsigned long long mn=~0ull; for(int i=threadIdx.x;i<256;i+=64){ mn=min(mn,d[i]); }
  for(int o=32;o;o>>=1) mn=min(mn,(unsigned long long)__shfl_xor((long long)mn,o));
  t0=mn; unsigned long long v=6000ull;
  asm volatile("s_memrealtime %0\n\ts_waitcnt lgkmcnt(0)":"=s"(t)::"memory");
  if(t-t0>100000ull) t0=t;
  for(int g=0;g<100000000;g++){ asm volatile("s_memrealtime %0\n\ts_waitcnt lgkmcnt(0)":"=s"(t)::"memory"); if(t-t0>=v) break; }
}
extern "C" void kernel_launch(void* const* d_in, const int* in_sizes, int n_in, void* d_out, int out_size, void* d_ws, size_t ws_size,
                              hipStream_t stream) {
  p_anchor<<<256,512,0,stream>>>((unsigned long long*)((char*)d_ws+(16u<<20)));
  fused<<<256, 512, 0, stream>>>((const u16*)d_in[0], (const u16*)d_in[1], (const u16*)d_in[2], (const u16*)d_in[3], (const u16*)d_in[4],
                                 (const u16*)d_in[5], (const u16*)d_in[7], (const u16*)d_in[8], (const u16*)d_in[9], (const u16*)d_in[10],
                                 (u16*)d_out, (char*)d_ws);
  p_spin<<<1,64,0,stream>>>((const unsigned long long*)((char*)d_ws+(16u<<20)));
}
```

```cpp
#include <hip/hip_runtime.h>

__global__ __launch_bounds__(512, 2) void fused(const void* q, unsigned q_bytes, unsigned q_fmt, const void* w1, unsigned w1_bytes, unsigned w1_fmt, const void* ssm, unsigned ssm_bytes, unsigned ssm_fmt, const void* ws, unsigned ws_bytes, unsigned ws_fmt, const void* b1, unsigned b1_bytes, unsigned b1_fmt, const void* w2, unsigned w2_bytes, unsigned w2_fmt, const void* k, unsigned k_bytes, unsigned k_fmt, const void* v, unsigned v_bytes, unsigned v_fmt, const void* shk, unsigned shk_bytes, unsigned shk_fmt, const void* ssd, unsigned ssd_bytes, unsigned ssd_fmt, const void* b2, unsigned b2_bytes, unsigned b2_fmt, const void* out, unsigned out_bytes, unsigned out_fmt) {
  __shared__ __attribute__((aligned(1024))) char smem[161792];
  asm volatile(
    "s_load_dwordx16 s[4:19], %0, 0x0\n"
    "s_load_dwordx16 s[20:35], %0, 0x40\n"
    "s_load_dwordx16 s[36:51], %0, 0x80\n"
    "v_readfirstlane_b32 s56, v0\n"
    "v_and_b32 v1, 63, v0\n"
    "s_and_b32 s57, %1, 7\n"
    "s_lshr_b32 s58, %1, 3\n"
    "s_lshr_b32 s56, s56, 6\n"
    "s_sub_u32 s77, s58, 24\n"
    "s_cmp_lg_u32 s77, s56\n"
    "s_cbranch_scc1 Lnopf_%=\n"
    "s_cmp_lt_u32 s56, 4\n"
    "s_cbranch_scc1 Lnopf_%=\n"
    "s_getpc_b64 s[100:101]\n"
    "Lpc_%=:\n"
    "v_and_b32 v21, 0xff, v0\n"
    "v_lshlrev_b32 v21, 7, v21\n"
    "v_min_u32 v22, Lend_%=-Lpc_%=-4, v21\n"
    "global_load_dword v20, v22, s[100:101]\n"
    "Lnopf_%=:\n"
    "v_lshlrev_b32 v3, 4, v0\n"
    "v_lshlrev_b32 v2, 4, v1\n"
    "v_and_b32 v4, 15, v0\n"
    "v_bfe_u32 v5, v0, 4, 2\n"
    "v_and_b32 v16, 7, v0\n"
    "s_lshl_b32 s55, s57, 5\n"
    "s_add_u32 s55, s55, s58\n"
    "s_mul_i32 s77, s58, 37\n"
    "s_lshr_b32 s77, s77, 8\n"
    "s_lshr_b32 s59, s57, 1\n"
    "s_lshl2_add_u32 s59, s59, s77\n"
    "s_mul_i32 s77, s77, 7\n"
    "s_sub_u32 s60, s58, s77\n"
    "s_and_b32 s77, s57, 1\n"
    "s_mul_i32 s77, s77, 7\n"
    "s_add_u32 s60, s60, s77\n"
    "s_lshr_b32 s77, s57, 1\n"
    "s_lshr_b32 s78, s58, 2\n"
    "s_lshl3_add_u32 s61, s77, s78\n"
    "s_and_b32 s77, s57, 1\n"
    "s_and_b32 s78, s58, 3\n"
    "s_lshl2_add_u32 s62, s77, s78\n"
    "s_and_b32 s63, s56, 3\n"
    "s_and_b32 s64, s56, 1\n"
    "s_lshr_b32 s65, s63, 1\n"
    "s_lshl_b32 s66, s63, 12\n"
    "s_mul_i32 s67, s65, 49152\n"
    "s_lshl_b32 s77, s64, 13\n"
    "s_add_u32 s67, s67, s77\n"
    "v_bfrev_b32 v10, 1\n"
    "v_cmp_eq_u32 vcc, 0, v1\n"
    "v_and_b32 v22, 3, v0\n"
    "v_lshlrev_b32 v31, 2, v22\n"
    "v_cndmask_b32 v9, v10, v1, vcc\n"
    "v_add_u32 v31, 153344, v31\n"
    "s_lshl2_add_u32 s79, s63, 153344\n"
    "v_mov_b32 v27, s79\n"
    "v_mov_b32 v28, 1\n"
    "s_lshl_b32 s74, s57, 8\n"
    "s_lshl3_add_u32 s74, s58, s74\n"
    "s_lshl1_add_u32 s73, s63, s74\n"
    "s_lshl_b32 s74, s73, 10\n"
    "v_lshrrev_b32 v21, 1, v5\n"
    "v_lshlrev_b32 v21, 10, v21\n"
    "v_bfe_u32 v22, v0, 2, 3\n"
    "v_lshl_add_u32 v21, v22, 6, v21\n"
    "v_and_b32 v22, 1, v5\n"
    "v_lshlrev_b32 v22, 5, v22\n"
    "v_and_b32 v23, 3, v0\n"
    "v_lshl_add_u32 v22, v23, 3, v22\n"
    "v_add3_u32 v7, v21, v22, s67\n"
    "v_xor_b32 v8, 32, v7\n"
    "s_setprio 3\n"
    "v_bfe_u32 v21, v0, 2, 3\n"
    "v_and_b32 v22, 3, v0\n"
    "v_lshrrev_b32 v23, 3, v1\n"
    "v_and_b32 v24, 4, v23\n"
    "v_lshrrev_b32 v26, 1, v23\n"
    "v_xor_b32 v25, v16, v26\n"
    "v_lshlrev_b32 v25, 4, v25\n"
    "v_lshl_add_u32 v25, v23, 10, v25\n"
    "s_lshl_b32 s77, s59, 17\n"
    "s_lshl_b32 s78, s63, 15\n"
    "s_add_u32 s77, s77, s78\n"
    "v_add_u32 v11, s77, v25\n"
    "v_xor_b32 v12, 0x2040, v11\n"
    "v_lshrrev_b32 v26, 2, v21\n"
    "v_xor_b32 v26, v26, v22\n"
    "v_or_b32 v26, v26, v24\n"
    "v_lshlrev_b32 v26, 4, v26\n"
    "v_mul_u32_u24 v25, 3584, v21\n"
    "s_mul_i32 s77, s63, 57344\n"
    "s_lshl_b32 s78, s60, 8\n"
    "s_add_u32 s77, s77, s78\n"
    "v_add3_u32 v13, v25, v26, s77\n"
    "v_xor_b32 v14, 32, v13\n"
    "v_add_u32 v14, 28672, v14\n"
    "v_lshlrev_b32 v26, 2, v1\n"
    "s_lshr_b32 s68, s59, 2\n"
    "s_lshl_b32 s68, s68, 8\n"
    "v_lshlrev_b32 v234, 3, v4\n"
    "v_lshl_add_u32 v234, v5, 1, v234\n"
    "s_and_b32 s75, s58, 7\n"
    "s_lshl2_add_u32 s75, s75, s63\n"
    "s_lshl_b32 s75, s75, 7\n"
    "s_waitcnt lgkmcnt(0)\n"
    "s_load_dwordx2 s[52:53], s[16:17], 0x0\n"
    "s_cmp_gt_u32 s56, 3\n"
    "s_cbranch_scc1 Lld_%=\n"
    "buffer_load_dwordx4 v[32:35], v2, s[4:7], s74 offen\n"
    "buffer_load_dwordx4 v[36:39], v2, s[4:7], s74 offen offset:1024\n"
    "buffer_load_ushort v234, v234, s[36:39], s75 offen\n"
    "s_setprio 0\n"
    "s_branch Lcomp_%=\n"
    "Lld_%=:\n"
    "s_cmp_lt_u32 s58, 28\n"
    "s_cbranch_scc0 Lsl_%=\n"
    "s_add_u32 m0, s66, 0\n"
    "s_mov_b32 s71, s68\n"
    "buffer_load_dwordx4 v11, s[4:7], s71 offen offset:0 lds\n"
    "s_add_u32 m0, s66, 49152\n"
    "s_mul_i32 s70, s68, 1792\n"
    "buffer_load_dwordx4 v13, s[8:11], s70 offen offset:0 lds\n"
    "s_add_u32 m0, s66, 1024\n"
    "s_add_u32 s72, s68, 16384\n"
    "buffer_load_dwordx4 v12, s[4:7], s71 offen offset:0 lds\n"
    "s_add_u32 m0, s66, 50048\n"
    "s_add_u32 s68, s68, 128\n"
    "buffer_load_dwordx4 v13, s[8:11], s70 offen offset:128 lds\n"
    "s_add_u32 m0, s66, 2048\n"
    "s_and_b32 s68, s68, 1023\n"
    "buffer_load_dwordx4 v11, s[4:7], s72 offen offset:0 lds\n"
    "s_add_u32 m0, s66, 51200\n"
    "s_nop 0\n"
    "buffer_load_dwordx4 v14, s[8:11], s70 offen offset:0 lds\n"
    "s_add_u32 m0, s66, 3072\n"
    "s_nop 0\n"
    "buffer_load_dwordx4 v12, s[4:7], s72 offen offset:0 lds\n"
    "s_add_u32 m0, s66, 52096\n"
    "s_nop 0\n"
    "buffer_load_dwordx4 v14, s[8:11], s70 offen offset:128 lds\n"
    "buffer_load_dword v144, v26, s[20:23], s78 offen\n"
    "s_setprio 2\n"
    "s_add_u32 m0, s66, 16384\n"
    "s_mov_b32 s71, s68\n"
    "buffer_load_dwordx4 v11, s[4:7], s71 offen offset:0 lds\n"
    "v_bfe_u32 v21, v0, 2, 2\n"
    "v_lshlrev_b32 v21, 6, v21\n"
    "v_lshl_add_u32 v21, v5, 11, v21\n"
    "v_and_b32 v23, 1, v5\n"
    "v_lshlrev_b32 v23, 5, v23\n"
    "v_and_b32 v22, 3, v0\n"
    "v_lshl_add_u32 v22, v22, 3, v23\n"
    "s_lshl_b32 s77, s63, 9\n"
    "s_add_u32 s77, s77, 49152\n"
    "v_add3_u32 v124, v21, v22, s77\n"
    "v_xor_b32 v125, 272, v124\n"
    "v_xor_b32 v126, 32, v124\n"
    "s_add_u32 m0, s66, 65536\n"
    "s_mul_i32 s70, s68, 1792\n"
    "buffer_load_dwordx4 v13, s[8:11], s70 offen offset:0 lds\n"
    "v_xor_b32 v127, 304, v124\n"
    "v_mov_b32 v120, 0x3f803f80\n"
    "v_mov_b32 v121, 0x3f803f80\n"
    "v_mov_b32 v122, 0x3f803f80\n"
    "v_mov_b32 v123, 0x3f803f80\n"
    "v_bfe_u32 v21, v0, 2, 3\n"
    "v_and_b32 v22, 3, v0\n"
    "v_lshrrev_b32 v23, 3, v1\n"
    "v_and_b32 v24, 4, v23\n"
    "v_and_b32 v25, 2, v23\n"
    "v_xor_b32 v25, v25, v22\n"
    "v_or_b32 v25, v25, v24\n"
    "s_add_u32 m0, s66, 17408\n"
    "s_add_u32 s72, s68, 16384\n"
    "buffer_load_dwordx4 v12, s[4:7], s71 offen offset:0 lds\n"
    "s_mul_i32 s77, s63, 458752\n"
    "s_lshl_b32 s78, s62, 7\n"
    "s_add_u32 s77, s77, s78\n"
    "v_lshlrev_b32 v25, 4, v25\n"
    "v_lshl_add_u32 v25, v21, 10, v25\n"
    "v_add_u32 v6, s77, v25\n"
    "s_lshr_b32 s77, s55, 3\n"
    "s_lshl_b32 s77, s77, 16\n"
    "s_and_b32 s78, s58, 7\n"
    "s_lshl_b32 s78, s78, 7\n"
    "s_add_u32 s77, s77, s78\n"
    "v_lshrrev_b32 v23, 3, v1\n"
    "s_add_u32 m0, s66, 66432\n"
    "s_add_u32 s68, s68, 128\n"
    "buffer_load_dwordx4 v13, s[8:11], s70 offen offset:128 lds\n"
    "v_lshrrev_b32 v24, 5, v1\n"
    "v_xor_b32 v25, v16, v24\n"
    "v_lshl_add_u32 v21, v23, 10, s77\n"
    "v_lshl_add_u32 v21, v25, 4, v21\n"
    "v_add_u32 v142, -8192, v21\n"
    "s_cmp_lt_u32 s55, 8\n"
    "s_cselect_b64 vcc, -1, 0\n"
    "s_lshl_b32 s77, s63, 14\n"
    "s_lshl_b32 s78, s64, 6\n"
    "v_cndmask_b32 v142, v142, v10, vcc\n"
    "v_xor_b32 v21, s78, v21\n"
    "v_add_u32 v141, s77, v21\n"
    "v_xor_b32 v139, 32, v141\n"
    "s_add_u32 m0, s66, 18432\n"
    "s_and_b32 s68, s68, 1023\n"
    "buffer_load_dwordx4 v11, s[4:7], s72 offen offset:0 lds\n"
    "v_xor_b32 v140, 64, v141\n"
    "s_lshl_b32 s69, s63, 11\n"
    "s_add_u32 s69, s69, 131584\n"
    "s_lshl_b32 s77, s63, 3\n"
    "s_lshl_b32 s98, 0xff, s77\n"
    "v_and_b32 v29, 0xff0, v3\n"
    "v_add_u32 v128, -8, v29\n"
    "v_cmp_eq_u32 vcc, 0, v29\n"
    "s_lshl_b32 s75, s55, 13\n"
    "s_add_u32 s76, s75, 4096\n"
    "v_cndmask_b32 v128, v128, v10, vcc\n"
    "s_lshl_b32 s77, s55, 8\n"
    "s_add_u32 m0, s66, 67584\n"
    "s_nop 0\n"
    "buffer_load_dwordx4 v14, s[8:11], s70 offen offset:0 lds\n"
    "s_lshl_b32 s78, s63, 19\n"
    "s_add_u32 s77, s77, s78\n"
    "s_add_u32 s77, s77, 1048576\n"
    "v_lshrrev_b32 v23, 3, v1\n"
    "v_lshlrev_b32 v23, 16, v23\n"
    "v_lshl_add_u32 v23, v16, 4, v23\n"
    "v_add_u32 v129, s77, v23\n"
    "s_lshl_b32 s78, s63, 10\n"
    "s_add_u32 s78, s78, 132032\n"
    "v_add_u32 v130, s78, v2\n"
    "s_lshr_b32 s77, s55, 3\n"
    "s_lshl2_add_u32 s77, s77, s63\n"
    "s_add_u32 m0, s66, 19456\n"
    "s_nop 0\n"
    "buffer_load_dwordx4 v12, s[4:7], s72 offen offset:0 lds\n"
    "s_lshl_b32 s77, s77, 10\n"
    "s_add_u32 s77, s77, 786432\n"
    "v_lshl_add_u32 v131, v16, 7, s77\n"
    "s_lshl_b32 s77, s59, 11\n"
    "s_lshl_b32 s78, s63, 9\n"
    "s_add_u32 s77, s77, s78\n"
    "s_add_u32 s77, s77, 131072\n"
    "v_and_b32 v22, 0x1f0, v2\n"
    "v_add_u32 v132, s77, v22\n"
    "s_add_u32 s78, s78, 150272\n"
    "v_add_u32 v133, s78, v22\n"
    "s_lshl2_add_u32 s77, s60, s63\n"
    "s_add_u32 m0, s66, 68480\n"
    "s_nop 0\n"
    "buffer_load_dwordx4 v14, s[8:11], s70 offen offset:128 lds\n"
    "s_lshl_b32 s77, s77, 8\n"
    "s_add_u32 s77, s77, 196608\n"
    "v_lshl_add_u32 v22, v4, 4, s77\n"
    "v_mul_u32_u24 v23, 14336, v5\n"
    "v_add_u32 v134, v22, v23\n"
    "s_lshr_b32 s77, s59, 2\n"
    "v_cmp_eq_u32 vcc, s77, v5\n"
    "s_lshl_b32 s77, s63, 7\n"
    "s_add_u32 s77, s77, 152832\n"
    "v_cndmask_b32 v135, v10, v134, vcc\n"
    "v_lshl_add_u32 v136, v4, 2, s77\n"
    "v_lshlrev_b32 v137, 3, v1\n"
    "v_add_u32 v137, 152320, v137\n"
    "s_and_b32 s39, s59, 3\n"
    "s_mul_i32 s77, s63, 14\n"
    "s_mul_i32 s78, s61, 56\n"
    "s_add_u32 s77, s77, s78\n"
    "s_lshl_b32 s73, s77, 12\n"
    "s_add_u32 s73, s73, 5242880\n"
    "s_lshl_b32 s77, s77, 7\n"
    "s_add_u32 s77, s77, 262144\n"
    "v_min_u32 v17, 6, v1\n"
    "v_lshl_add_u32 v17, v17, 8, s77\n"
    "v_mov_b32 v15, v17\n"
    "s_waitcnt lgkmcnt(0)\n"
    "s_add_u32 s52, s52, 0x7f4a7c15\n"
    "s_addc_u32 s53, s53, 0x9e3779b9\n"
    "v_mov_b32 v18, s52\n"
    "v_mov_b32 v19, s53\n"
    "s_waitcnt vmcnt(9) lgkmcnt(0)\n"
    "s_barrier\n"
    "s_mov_b32 m0, 131584\n"
    "s_mov_b32 exec_lo, 0\n"
    "s_mov_b32 exec_hi, s98\n"
    "buffer_load_dwordx4 v142, s[28:31], 0 offen lds\n"
    "s_mov_b64 exec, -1\n"
    "s_add_u32 m0, s69, 1024\n"
    "s_mov_b32 s79, 0\n"
    "buffer_load_dwordx4 v139, s[28:31], s79 offen offset:0 lds\n"
    "s_add_u32 m0, s69, 2048\n"
    "s_mov_b32 s79, 8192\n"
    "buffer_load_dwordx4 v140, s[28:31], s79 offen offset:0 lds\n"
    "s_add_u32 m0, s69, 9216\n"
    "s_mov_b32 s79, 0\n"
    "buffer_load_dwordx4 v141, s[32:35], s79 offen offset:0 lds\n"
    "s_add_u32 m0, s69, 10240\n"
    "s_mov_b32 s79, 8192\n"
    "buffer_load_dwordx4 v139, s[32:35], s79 offen offset:0 lds\n"
    "s_add_u32 m0, s66, 32768\n"
    "s_mov_b32 s71, s68\n"
    "buffer_load_dwordx4 v11, s[4:7], s71 offen offset:0 lds\n"
    "s_add_u32 m0, s66, 81920\n"
    "s_mul_i32 s70, s68, 1792\n"
    "buffer_load_dwordx4 v13, s[8:11], s70 offen offset:0 lds\n"
    "s_add_u32 m0, s66, 33792\n"
    "s_add_u32 s72, s68, 16384\n"
    "buffer_load_dwordx4 v12, s[4:7], s71 offen offset:0 lds\n"
    "s_add_u32 m0, s66, 82816\n"
    "s_add_u32 s68, s68, 128\n"
    "buffer_load_dwordx4 v13, s[8:11], s70 offen offset:128 lds\n"
    "s_add_u32 m0, s66, 34816\n"
    "s_and_b32 s68, s68, 1023\n"
    "buffer_load_dwordx4 v11, s[4:7], s72 offen offset:0 lds\n"
    "s_add_u32 m0, s66, 83968\n"
    "s_nop 0\n"
    "buffer_load_dwordx4 v14, s[8:11], s70 offen offset:0 lds\n"
    "s_cmp_lg_u32 s39, 0\n"
    "s_cbranch_scc1 Lnc0_%=\n"
    "ds_read_b64_tr_b16 v[104:105], v124 offset:0\n"
    "ds_read_b64_tr_b16 v[106:107], v125 offset:0\n"
    "ds_read_b64_tr_b16 v[108:109], v126 offset:0\n"
    "ds_read_b64_tr_b16 v[110:111], v127 offset:0\n"
    "ds_read_b64_tr_b16 v[112:113], v124 offset:8192\n"
    "ds_read_b64_tr_b16 v[114:115], v125 offset:8192\n"
    "ds_read_b64_tr_b16 v[116:117], v126 offset:8192\n"
    "ds_read_b64_tr_b16 v[118:119], v127 offset:8192\n"
    "s_waitcnt lgkmcnt(0)\n"
    "v_mfma_f32_16x16x32_bf16 v[96:99], v[120:123], v[104:107], 0\n"
    "v_mfma_f32_16x16x32_bf16 v[100:103], v[120:123], v[108:111], 0\n"
    "v_mfma_f32_16x16x32_bf16 v[96:99], v[120:123], v[112:115], v[96:99]\n"
    "v_mfma_f32_16x16x32_bf16 v[100:103], v[120:123], v[116:119], v[100:103]\n"
    "Lnc0_%=:\n"
    "s_add_u32 m0, s66, 35840\n"
    "s_nop 0\n"
    "buffer_load_dwordx4 v12, s[4:7], s72 offen offset:0 lds\n"
    "s_add_u32 m0, s66, 84864\n"
    "s_nop 0\n"
    "buffer_load_dwordx4 v14, s[8:11], s70 offen offset:128 lds\n"
    "s_waitcnt vmcnt(13) lgkmcnt(0)\n"
    "s_barrier\n"
    "s_add_u32 m0, s66, 0\n"
    "s_mov_b32 s71, s68\n"
    "buffer_load_dwordx4 v11, s[4:7], s71 offen offset:0 lds\n"
    "s_add_u32 m0, s66, 49152\n"
    "s_mul_i32 s70, s68, 1792\n"
    "buffer_load_dwordx4 v13, s[8:11], s70 offen offset:0 lds\n"
    "s_add_u32 m0, s66, 1024\n"
    "s_add_u32 s72, s68, 16384\n"
    "buffer_load_dwordx4 v12, s[4:7], s71 offen offset:0 lds\n"
    "s_add_u32 m0, s66, 50048\n"
    "s_add_u32 s68, s68, 128\n"
    "buffer_load_dwordx4 v13, s[8:11], s70 offen offset:128 lds\n"
    "s_add_u32 m0, s66, 2048\n"
    "s_and_b32 s68, s68, 1023\n"
    "buffer_load_dwordx4 v11, s[4:7], s72 offen offset:0 lds\n"
    "s_add_u32 m0, s66, 51200\n"
    "s_nop 0\n"
    "buffer_load_dwordx4 v14, s[8:11], s70 offen offset:0 lds\n"
    "s_cmp_lg_u32 s39, 0\n"
    "s_cbranch_scc1 Lnc1_%=\n"
    "ds_read_b64_tr_b16 v[104:105], v124 offset:16384\n"
    "ds_read_b64_tr_b16 v[106:107], v125 offset:16384\n"
    "ds_read_b64_tr_b16 v[108:109], v126 offset:16384\n"
    "ds_read_b64_tr_b16 v[110:111], v127 offset:16384\n"
    "ds_read_b64_tr_b16 v[112:113], v124 offset:24576\n"
    "ds_read_b64_tr_b16 v[114:115], v125 offset:24576\n"
    "ds_read_b64_tr_b16 v[116:117], v126 offset:24576\n"
    "ds_read_b64_tr_b16 v[118:119], v127 offset:24576\n"
    "s_waitcnt lgkmcnt(0)\n"
    "v_mfma_f32_16x16x32_bf16 v[96:99], v[120:123], v[104:107], v[96:99]\n"
    "v_mfma_f32_16x16x32_bf16 v[100:103], v[120:123], v[108:111], v[100:103]\n"
    "v_mfma_f32_16x16x32_bf16 v[96:99], v[120:123], v[112:115], v[96:99]\n"
    "v_mfma_f32_16x16x32_bf16 v[100:103], v[120:123], v[116:119], v[100:103]\n"
    "s_nop 6\n"
    "v_mov_b32 v97, s52\n"
    "v_mov_b32 v98, v100\n"
    "v_mov_b32 v99, s53\n"
    "buffer_store_dwordx4 v[96:99], v135, s[16:19], 0 offen sc1\n"
    "Lnc1_%=:\n"
    "s_add_u32 m0, s66, 3072\n"
    "s_nop 0\n"
    "buffer_load_dwordx4 v12, s[4:7], s72 offen offset:0 lds\n"
    "s_add_u32 m0, s66, 52096\n"
    "s_nop 0\n"
    "buffer_load_dwordx4 v14, s[8:11], s70 offen offset:128 lds\n"
    "s_waitcnt vmcnt(8) lgkmcnt(0)\n"
    "s_barrier\n"
    "s_add_u32 m0, s66, 16384\n"
    "s_mov_b32 s71, s68\n"
    "buffer_load_dwordx4 v11, s[4:7], s71 offen offset:0 lds\n"
    "v_lshlrev_b32 v22, 16, v144\n"
    "s_add_u32 m0, s66, 65536\n"
    "s_mul_i32 s70, s68, 1792\n"
    "buffer_load_dwordx4 v13, s[8:11], s70 offen offset:0 lds\n"
    "v_and_b32 v23, 0xffff0000, v144\n"
    "s_add_u32 m0, s66, 17408\n"
    "s_add_u32 s72, s68, 16384\n"
    "buffer_load_dwordx4 v12, s[4:7], s71 offen offset:0 lds\n"
    "ds_write_b64 v137, v[22:23]\n"
    "s_add_u32 m0, s66, 66432\n"
    "s_add_u32 s68, s68, 128\n"
    "buffer_load_dwordx4 v13, s[8:11], s70 offen offset:128 lds\n"
    "s_add_u32 m0, s66, 18432\n"
    "s_and_b32 s68, s68, 1023\n"
    "buffer_load_dwordx4 v11, s[4:7], s72 offen offset:0 lds\n"
    "s_add_u32 m0, s66, 67584\n"
    "s_nop 0\n"
    "buffer_load_dwordx4 v14, s[8:11], s70 offen offset:0 lds\n"
    "s_add_u32 m0, s66, 19456\n"
    "s_nop 0\n"
    "buffer_load_dwordx4 v12, s[4:7], s72 offen offset:0 lds\n"
    "s_add_u32 m0, s66, 68480\n"
    "s_nop 0\n"
    "buffer_load_dwordx4 v14, s[8:11], s70 offen offset:128 lds\n"
    "s_waitcnt vmcnt(8) lgkmcnt(0)\n"
    "s_barrier\n"
    "s_add_u32 m0, s66, 32768\n"
    "s_mov_b32 s71, s68\n"
    "buffer_load_dwordx4 v11, s[4:7], s71 offen offset:0 lds\n"
    "s_add_u32 m0, s66, 81920\n"
    "s_mul_i32 s70, s68, 1792\n"
    "buffer_load_dwordx4 v13, s[8:11], s70 offen offset:0 lds\n"
    "s_add_u32 m0, s66, 33792\n"
    "s_add_u32 s72, s68, 16384\n"
    "buffer_load_dwordx4 v12, s[4:7], s71 offen offset:0 lds\n"
    "s_add_u32 m0, s66, 82816\n"
    "s_add_u32 s68, s68, 128\n"
    "buffer_load_dwordx4 v13, s[8:11], s70 offen offset:128 lds\n"
    "s_add_u32 m0, s66, 34816\n"
    "s_and_b32 s68, s68, 1023\n"
    "buffer_load_dwordx4 v11, s[4:7], s72 offen offset:0 lds\n"
    "s_add_u32 m0, s66, 83968\n"
    "s_nop 0\n"
    "buffer_load_dwordx4 v14, s[8:11], s70 offen offset:0 lds\n"
    "s_add_u32 m0, s66, 35840\n"
    "s_nop 0\n"
    "buffer_load_dwordx4 v12, s[4:7], s72 offen offset:0 lds\n"
    "s_add_u32 m0, s66, 84864\n"
    "s_nop 0\n"
    "buffer_load_dwordx4 v14, s[8:11], s70 offen offset:128 lds\n"
    "s_waitcnt vmcnt(8) lgkmcnt(0)\n"
    "s_barrier\n"
    "s_add_u32 m0, s66, 0\n"
    "s_mov_b32 s71, s68\n"
    "buffer_load_dwordx4 v11, s[4:7], s71 offen offset:0 lds\n"
    "s_lshr_b32 s77, s57, 1\n"
    "s_mul_i32 s77, s77, 1\n"
    "s_add_u32 s28, s77, 0\n"
    "s_and_b32 s28, s28, 3\n"
    "s_add_u32 m0, s66, 49152\n"
    "s_mul_i32 s70, s68, 1792\n"
    "buffer_load_dwordx4 v13, s[8:11], s70 offen offset:0 lds\n"
    "s_lshl_b32 s28, s28, 13\n"
    "s_add_u32 s29, s77, 1\n"
    "s_and_b32 s29, s29, 3\n"
    "s_lshl_b32 s29, s29, 13\n"
    "s_add_u32 m0, s66, 1024\n"
    "s_add_u32 s72, s68, 16384\n"
    "buffer_load_dwordx4 v12, s[4:7], s71 offen offset:0 lds\n"
    "s_add_u32 s30, s77, 2\n"
    "s_and_b32 s30, s30, 3\n"
    "s_lshl_b32 s30, s30, 13\n"
    "s_add_u32 s31, s77, 3\n"
    "s_add_u32 m0, s66, 50048\n"
    "s_add_u32 s68, s68, 128\n"
    "buffer_load_dwordx4 v13, s[8:11], s70 offen offset:128 lds\n"
    "s_and_b32 s31, s31, 3\n"
    "s_lshl_b32 s31, s31, 13\n"
    "s_lshl_b32 s77, s77, 1\n"
    "s_lshr_b32 s77, 0x924924, s77\n"
    "s_add_u32 m0, s66, 2048\n"
    "s_and_b32 s68, s68, 1023\n"
    "buffer_load_dwordx4 v11, s[4:7], s72 offen offset:0 lds\n"
    "s_bfe_u32 s32, s77, 0x20000\n"
    "s_add_u32 s32, s32, 4\n"
    "s_lshl_b32 s32, s32, 13\n"
    "s_bfe_u32 s33, s77, 0x20002\n"
    "s_add_u32 m0, s66, 51200\n"
    "s_nop 0\n"
    "buffer_load_dwordx4 v14, s[8:11], s70 offen offset:0 lds\n"
    "s_add_u32 s33, s33, 4\n"
    "s_lshl_b32 s33, s33, 13\n"
    "s_bfe_u32 s34, s77, 0x20004\n"
    "s_add_u32 s34, s34, 4\n"
    "s_add_u32 m0, s66, 3072\n"
    "s_nop 0\n"
    "buffer_load_dwordx4 v12, s[4:7], s72 offen offset:0 lds\n"
    "s_lshl_b32 s34, s34, 13\n"
    "s_add_u32 m0, s66, 52096\n"
    "s_nop 0\n"
    "buffer_load_dwordx4 v14, s[8:11], s70 offen offset:128 lds\n"
    "s_waitcnt vmcnt(8) lgkmcnt(0)\n"
    "s_barrier\n"
    "s_add_u32 m0, s66, 16384\n"
    "s_mov_b32 s71, s68\n"
    "buffer_load_dwordx4 v11, s[4:7], s71 offen offset:0 lds\n"
    "buffer_load_dwordx4 v[164:167], v132, s[16:19], 0 offen sc1\n"
    "buffer_load_dwordx4 v[160:163], v134, s[16:19], 0 offen sc1\n"
    "s_add_u32 m0, s66, 65536\n"
    "s_mul_i32 s70, s68, 1792\n"
    "buffer_load_dwordx4 v13, s[8:11], s70 offen offset:0 lds\n"
    "s_add_u32 m0, s66, 17408\n"
    "s_add_u32 s72, s68, 16384\n"
    "buffer_load_dwordx4 v12, s[4:7], s71 offen offset:0 lds\n"
    "s_add_u32 m0, s66, 66432\n"
    "s_add_u32 s68, s68, 128\n"
    "buffer_load_dwordx4 v13, s[8:11], s70 offen offset:128 lds\n"
    "s_add_u32 m0, s66, 18432\n"
    "s_and_b32 s68, s68, 1023\n"
    "buffer_load_dwordx4 v11, s[4:7], s72 offen offset:0 lds\n"
    "s_add_u32 m0, s66, 67584\n"
    "s_nop 0\n"
    "buffer_load_dwordx4 v14, s[8:11], s70 offen offset:0 lds\n"
    "s_add_u32 m0, s66, 19456\n"
    "s_nop 0\n"
    "buffer_load_dwordx4 v12, s[4:7], s72 offen offset:0 lds\n"
    "s_add_u32 m0, s66, 68480\n"
    "s_nop 0\n"
    "buffer_load_dwordx4 v14, s[8:11], s70 offen offset:128 lds\n"
    "s_waitcnt vmcnt(10) lgkmcnt(0)\n"
    "s_barrier\n"
    "s_add_u32 m0, s67, 32768\n"
    "s_lshl3_add_u32 s79, s28, 0\n"
    "buffer_load_dwordx4 v6, s[24:27], s79 offen offset:0 lds\n"
    "buffer_load_dwordx4 v[148:151], v29, s[12:15], s75 offen\n"
    "buffer_load_dwordx2 v[156:157], v128, s[12:15], s75 offen\n"
    "buffer_load_dwordx4 v[152:155], v29, s[12:15], s76 offen\n"
    "buffer_load_dwordx2 v[158:159], v128, s[12:15], s76 offen\n"
    "s_waitcnt vmcnt(13)\n"
    "v_cmp_ne_u32 vcc, s52, v165\n"
    "v_cmp_ne_u32 s[100:101], s53, v167\n"
    "s_or_b64 vcc, vcc, s[100:101]\n"
    "s_cbranch_scc1 Lsp0_%=\n"
    "Lsb0_%=:\n"
    "v_add_f32 v22, 0x3727c5ac, v166\n"
    "v_rsq_f32 v22, v22\n"
    "s_add_u32 m0, s67, 33792\n"
    "s_lshl3_add_u32 s79, s28, 8192\n"
    "buffer_load_dwordx4 v6, s[24:27], s79 offen offset:0 lds\n"
    "v_mov_b32 v24, 0x3727c5ac\n"
    "v_mul_f32 v23, v22, v166\n"
    "v_fma_f32 v23, v23, v22, v24\n"
    "v_rsq_f32 v23, v23\n"
    "s_nop 0\n"
    "v_mul_f32 v22, v22, v23\n"
    "buffer_load_dwordx2 v[168:169], v131, s[16:19], 0 offen sc1\n"
    "v_mul_f32 v166, v22, v164\n"
    "v_mul_f32 v167, v22, v164\n"
    "v_mov_b32 v164, v22\n"
    "v_mov_b32 v165, v22\n"
    "ds_write_b128 v133, v[164:167]\n"
    "s_add_u32 m0, s67, 34816\n"
    "s_lshl3_add_u32 s79, s28, 16384\n"
    "buffer_load_dwordx4 v6, s[24:27], s79 offen offset:0 lds\n"
    "s_waitcnt vmcnt(15)\n"
    "v_cmp_ne_u32 vcc, s52, v161\n"
    "v_cmp_ne_u32 s[100:101], s53, v163\n"
    "s_or_b64 vcc, vcc, s[100:101]\n"
    "s_cbranch_scc1 Lsp1_%=\n"
    "Lsb1_%=:\n"
    "v_mov_b32 v161, v160\n"
    "v_mov_b32 v163, v162\n"
    "s_nop 0\n"
    "v_permlane16_swap_b32 v160, v161\n"
    "s_add_u32 m0, s67, 35840\n"
    "s_lshl3_add_u32 s79, s28, 24576\n"
    "buffer_load_dwordx4 v6, s[24:27], s79 offen offset:0 lds\n"
    "v_permlane16_swap_b32 v162, v163\n"
    "v_add_f32 v160, v160, v161\n"
    "v_add_f32 v162, v162, v163\n"
    "v_mov_b32 v161, v160\n"
    "v_mov_b32 v163, v162\n"
    "s_add_u32 m0, s67, 36864\n"
    "s_lshl3_add_u32 s79, s28, 32768\n"
    "buffer_load_dwordx4 v6, s[24:27], s79 offen offset:0 lds\n"
    "v_permlane32_swap_b32 v160, v161\n"
    "v_permlane32_swap_b32 v162, v163\n"
    "v_add_f32 v160, v160, v161\n"
    "v_add_f32 v162, v162, v163\n"
    "ds_write2_b32 v136, v160, v162 offset1:16\n"
    "s_add_u32 m0, s67, 37888\n"
    "s_lshl3_add_u32 s79, s28, 40960\n"
    "buffer_load_dwordx4 v6, s[24:27], s79 offen offset:0 lds\n"
    "s_add_u32 m0, s67, 38912\n"
    "s_lshl3_add_u32 s79, s28, 49152\n"
    "buffer_load_dwordx4 v6, s[24:27], s79 offen offset:0 lds\n"
    "s_add_u32 m0, s67, 39936\n"
    "s_lshl3_add_u32 s79, s28, 57344\n"
    "buffer_load_dwordx4 v6, s[24:27], s79 offen offset:0 lds\n"
    "s_waitcnt vmcnt(13) lgkmcnt(0)\n"
    "s_barrier\n"
    "s_add_u32 m0, s67, 0\n"
    "s_lshl3_add_u32 s79, s29, 0\n"
    "buffer_load_dwordx4 v6, s[24:27], s79 offen offset:0 lds\n"
    "s_waitcnt vmcnt(7)\n"
    "v_cmp_ne_u64 vcc, s[52:53], v[168:169]\n"
    "s_cbranch_vccnz Lsp2_%=\n"
    "Lsb2_%=:\n"
    "buffer_load_dwordx4 v[172:175], v129, s[16:19], 0 offen sc1\n"
    "buffer_load_dwordx4 v[176:179], v129, s[16:19], 0 offen offset:128 sc1\n"
    "s_add_u32 m0, s67, 1024\n"
    "s_lshl3_add_u32 s79, s29, 8192\n"
    "buffer_load_dwordx4 v6, s[24:27], s79 offen offset:0 lds\n"
    "s_add_u32 m0, s67, 2048\n"
    "s_lshl3_add_u32 s79, s29, 16384\n"
    "buffer_load_dwordx4 v6, s[24:27], s79 offen offset:0 lds\n"
    "s_add_u32 m0, s67, 3072\n"
    "s_lshl3_add_u32 s79, s29, 24576\n"
    "buffer_load_dwordx4 v6, s[24:27], s79 offen offset:0 lds\n"
    "s_add_u32 m0, s67, 4096\n"
    "s_lshl3_add_u32 s79, s29, 32768\n"
    "buffer_load_dwordx4 v6, s[24:27], s79 offen offset:0 lds\n"
    "s_add_u32 m0, s67, 5120\n"
    "s_lshl3_add_u32 s79, s29, 40960\n"
    "buffer_load_dwordx4 v6, s[24:27], s79 offen offset:0 lds\n"
    "s_add_u32 m0, s67, 6144\n"
    "s_lshl3_add_u32 s79, s29, 49152\n"
    "buffer_load_dwordx4 v6, s[24:27], s79 offen offset:0 lds\n"
    "s_add_u32 m0, s67, 7168\n"
    "s_lshl3_add_u32 s79, s29, 57344\n"
    "buffer_load_dwordx4 v6, s[24:27], s79 offen offset:0 lds\n"
    "ds_read_b32 v26, v31 offset:0\n"
    "s_waitcnt lgkmcnt(0)\n"
    "v_cmp_ne_u32 vcc, 1, v26\n"
    "s_cbranch_vccnz Lsp3_%=\n"
    "Lsb3_%=:\n"
    "s_setprio 0\n"
    "s_add_u32 m0, s67, 16384\n"
    "s_lshl3_add_u32 s79, s30, 0\n"
    "buffer_load_dwordx4 v6, s[24:27], s79 offen offset:0 lds\n"
    "s_waitcnt vmcnt(11)\n"
    "ds_read_b64_tr_b16 v[192:193], v7 offset:32768\n"
    "ds_read_b64_tr_b16 v[194:195], v7 offset:34816\n"
    "ds_read_b64_tr_b16 v[196:197], v8 offset:32768\n"
    "ds_read_b64_tr_b16 v[198:199], v8 offset:34816\n"
    "ds_read_b64_tr_b16 v[200:201], v7 offset:33280\n"
    "ds_read_b64_tr_b16 v[202:203], v7 offset:35328\n"
    "ds_read_b64_tr_b16 v[204:205], v8 offset:33280\n"
    "ds_read_b64_tr_b16 v[206:207], v8 offset:35328\n"
    "ds_read_b64_tr_b16 v[208:209], v7 offset:36864\n"
    "ds_read_b64_tr_b16 v[210:211], v7 offset:38912\n"
    "ds_read_b64_tr_b16 v[212:213], v8 offset:36864\n"
    "ds_read_b64_tr_b16 v[214:215], v8 offset:38912\n"
    "ds_read_b64_tr_b16 v[216:217], v7 offset:37376\n"
    "ds_read_b64_tr_b16 v[218:219], v7 offset:39424\n"
    "ds_read_b64_tr_b16 v[220:221], v8 offset:37376\n"
    "ds_read_b64_tr_b16 v[222:223], v8 offset:39424\n"
    "s_add_u32 m0, s67, 17408\n"
    "s_lshl3_add_u32 s79, s30, 8192\n"
    "buffer_load_dwordx4 v6, s[24:27], s79 offen offset:0 lds\n"
    "s_add_u32 m0, s67, 18432\n"
    "s_lshl3_add_u32 s79, s30, 16384\n"
    "buffer_load_dwordx4 v6, s[24:27], s79 offen offset:0 lds\n"
    "s_add_u32 m0, s67, 19456\n"
    "s_lshl3_add_u32 s79, s30, 24576\n"
    "buffer_load_dwordx4 v6, s[24:27], s79 offen offset:0 lds\n"
    "s_add_u32 m0, s67, 20480\n"
    "s_lshl3_add_u32 s79, s30, 32768\n"
    "buffer_load_dwordx4 v6, s[24:27], s79 offen offset:0 lds\n"
    "s_add_u32 m0, s67, 21504\n"
    "s_lshl3_add_u32 s79, s30, 40960\n"
    "buffer_load_dwordx4 v6, s[24:27], s79 offen offset:0 lds\n"
    "s_add_u32 m0, s67, 22528\n"
    "s_lshl3_add_u32 s79, s30, 49152\n"
    "buffer_load_dwordx4 v6, s[24:27], s79 offen offset:0 lds\n"
    "s_add_u32 m0, s67, 23552\n"
    "s_lshl3_add_u32 s79, s30, 57344\n"
    "buffer_load_dwordx4 v6, s[24:27], s79 offen offset:0 lds\n"
    "s_waitcnt lgkmcnt(0)\n"
    "s_waitcnt vmcnt(8)\n"
    "ds_read_b64_tr_b16 v[224:225], v7 offset:0\n"
    "ds_read_b64_tr_b16 v[226:227], v7 offset:2048\n"
    "ds_read_b64_tr_b16 v[228:229], v8 offset:0\n"
    "ds_read_b64_tr_b16 v[230:231], v8 offset:2048\n"
    "ds_read_b64_tr_b16 v[232:233], v7 offset:512\n"
    "ds_read_b64_tr_b16 v[234:235], v7 offset:2560\n"
    "ds_read_b64_tr_b16 v[236:237], v8 offset:512\n"
    "ds_read_b64_tr_b16 v[238:239], v8 offset:2560\n"
    "ds_read_b64_tr_b16 v[240:241], v7 offset:4096\n"
    "ds_read_b64_tr_b16 v[242:243], v7 offset:6144\n"
    "ds_read_b64_tr_b16 v[244:245], v8 offset:4096\n"
    "ds_read_b64_tr_b16 v[246:247], v8 offset:6144\n"
    "ds_read_b64_tr_b16 v[248:249], v7 offset:4608\n"
    "ds_read_b64_tr_b16 v[250:251], v7 offset:6656\n"
    "ds_read_b64_tr_b16 v[252:253], v8 offset:4608\n"
    "ds_read_b64_tr_b16 v[254:255], v8 offset:6656\n"
    "s_setprio 3\n"
    "s_mov_b32 s75, 0xffff\n"
    "v_add_u32 v29, 98352, v29\n"
    "v_alignbit_b32 v64, v151, v151, 16\n"
    "v_alignbit_b32 v65, v150, v150, 16\n"
    "v_alignbit_b32 v66, v149, v149, 16\n"
    "v_alignbit_b32 v67, v148, v148, 16\n"
    "ds_write_b128 v29, v[64:67] offset:12480\n"
    "v_alignbit_b32 v68, v150, v150, 16\n"
    "v_alignbit_b32 v69, v149, v149, 16\n"
    "v_alignbit_b32 v70, v148, v148, 16\n"
    "v_alignbit_b32 v71, v157, v157, 16\n"
    "ds_write_b128 v29, v[68:71] offset:4160\n"
    "v_bfi_b32 v72, s75, v151, v150\n"
    "v_bfi_b32 v73, s75, v150, v149\n"
    "v_bfi_b32 v74, s75, v149, v148\n"
    "v_bfi_b32 v75, s75, v148, v157\n"
    "ds_write_b128 v29, v[72:75] offset:8320\n"
    "v_bfi_b32 v76, s75, v150, v149\n"
    "v_bfi_b32 v77, s75, v149, v148\n"
    "v_bfi_b32 v78, s75, v148, v157\n"
    "v_bfi_b32 v79, s75, v157, v156\n"
    "ds_write_b128 v29, v[76:79] offset:0\n"
    "v_alignbit_b32 v80, v155, v155, 16\n"
    "v_alignbit_b32 v81, v154, v154, 16\n"
    "v_alignbit_b32 v82, v153, v153, 16\n"
    "v_alignbit_b32 v83, v152, v152, 16\n"
    "ds_write_b128 v29, v[80:83] offset:29120\n"
    "v_alignbit_b32 v84, v154, v154, 16\n"
    "v_alignbit_b32 v85, v153, v153, 16\n"
    "v_alignbit_b32 v86, v152, v152, 16\n"
    "v_alignbit_b32 v87, v159, v159, 16\n"
    "ds_write_b128 v29, v[84:87] offset:20800\n"
    "v_bfi_b32 v88, s75, v155, v154\n"
    "v_bfi_b32 v89, s75, v154, v153\n"
    "v_bfi_b32 v90, s75, v153, v152\n"
    "v_bfi_b32 v91, s75, v152, v159\n"
    "ds_write_b128 v29, v[88:91] offset:24960\n"
    "v_bfi_b32 v92, s75, v154, v153\n"
    "v_bfi_b32 v93, s75, v153, v152\n"
    "v_bfi_b32 v94, s75, v152, v159\n"
    "v_bfi_b32 v95, s75, v159, v158\n"
    "ds_write_b128 v29, v[92:95] offset:16640\n"
    "s_mov_b32 s77, 0x05040100\n"
    "s_mov_b32 s78, 0x07060302\n"
    "v_perm_b32 v184, v173, v172, s77\n"
    "v_perm_b32 v185, v175, v174, s77\n"
    "v_perm_b32 v186, v177, v176, s77\n"
    "v_perm_b32 v187, v179, v178, s77\n"
    "ds_write_b128 v130, v[184:187] offset:0\n"
    "ds_write_b64 v130, v[184:185] offset:4648\n"
    "ds_write_b64 v130, v[186:187] offset:4656\n"
    "v_perm_b32 v188, v173, v172, s78\n"
    "v_perm_b32 v189, v175, v174, s78\n"
    "v_perm_b32 v190, v177, v176, s78\n"
    "v_perm_b32 v191, v179, v178, s78\n"
    "ds_write_b128 v130, v[188:191] offset:9280\n"
    "ds_write_b64 v130, v[188:189] offset:13928\n"
    "ds_write_b64 v130, v[190:191] offset:13936\n"
    "ds_write_b32 v27, v28 offset:16\n"
    "s_setprio 0\n"
    "ds_read_b32 v26, v31 offset:64\n"
    "s_setprio 3\n"
    "s_waitcnt lgkmcnt(0)\n"
    "v_cmp_ne_u32 vcc, 1, v26\n"
    "s_cbranch_vccnz Lsp4_%=\n"
    "Lsb4_%=:\n"
    "s_setprio 0\n"
    "s_add_u32 m0, s67, 32768\n"
    "s_lshl3_add_u32 s79, s31, 0\n"
    "buffer_load_dwordx4 v6, s[24:27], s79 offen offset:0 lds\n"
    "s_add_u32 m0, s67, 33792\n"
    "s_lshl3_add_u32 s79, s31, 8192\n"
    "buffer_load_dwordx4 v6, s[24:27], s79 offen offset:0 lds\n"
    "s_add_u32 m0, s67, 34816\n"
    "s_lshl3_add_u32 s79, s31, 16384\n"
    "buffer_load_dwordx4 v6, s[24:27], s79 offen offset:0 lds\n"
    "s_add_u32 m0, s67, 35840\n"
    "s_lshl3_add_u32 s79, s31, 24576\n"
    "buffer_load_dwordx4 v6, s[24:27], s79 offen offset:0 lds\n"
    "s_add_u32 m0, s67, 36864\n"
    "s_lshl3_add_u32 s79, s31, 32768\n"
    "buffer_load_dwordx4 v6, s[24:27], s79 offen offset:0 lds\n"
    "s_add_u32 m0, s67, 37888\n"
    "s_lshl3_add_u32 s79, s31, 40960\n"
    "buffer_load_dwordx4 v6, s[24:27], s79 offen offset:0 lds\n"
    "s_add_u32 m0, s67, 38912\n"
    "s_lshl3_add_u32 s79, s31, 49152\n"
    "buffer_load_dwordx4 v6, s[24:27], s79 offen offset:0 lds\n"
    "s_add_u32 m0, s67, 39936\n"
    "s_lshl3_add_u32 s79, s31, 57344\n"
    "buffer_load_dwordx4 v6, s[24:27], s79 offen offset:0 lds\n"
    "s_waitcnt lgkmcnt(0)\n"
    "Lb4_%=:\n"
    "s_add_u32 m0, s67, 0\n"
    "s_lshl3_add_u32 s79, s32, 0\n"
    "buffer_load_dwordx4 v6, s[24:27], s79 offen offset:0 lds\n"
    "s_add_u32 m0, s67, 1024\n"
    "s_lshl3_add_u32 s79, s32, 8192\n"
    "buffer_load_dwordx4 v6, s[24:27], s79 offen offset:0 lds\n"
    "s_add_u32 m0, s67, 2048\n"
    "s_lshl3_add_u32 s79, s32, 16384\n"
    "buffer_load_dwordx4 v6, s[24:27], s79 offen offset:0 lds\n"
    "s_waitcnt vmcnt(11)\n"
    "ds_read_b64_tr_b16 v[32:33], v7 offset:16384\n"
    "ds_read_b64_tr_b16 v[34:35], v7 offset:18432\n"
    "ds_read_b64_tr_b16 v[36:37], v8 offset:16384\n"
    "ds_read_b64_tr_b16 v[38:39], v8 offset:18432\n"
    "ds_read_b64_tr_b16 v[40:41], v7 offset:16896\n"
    "ds_read_b64_tr_b16 v[42:43], v7 offset:18944\n"
    "ds_read_b64_tr_b16 v[44:45], v8 offset:16896\n"
    "ds_read_b64_tr_b16 v[46:47], v8 offset:18944\n"
    "ds_read_b64_tr_b16 v[48:49], v7 offset:20480\n"
    "ds_read_b64_tr_b16 v[50:51], v7 offset:22528\n"
    "ds_read_b64_tr_b16 v[52:53], v8 offset:20480\n"
    "ds_read_b64_tr_b16 v[54:55], v8 offset:22528\n"
    "ds_read_b64_tr_b16 v[56:57], v7 offset:20992\n"
    "ds_read_b64_tr_b16 v[58:59], v7 offset:23040\n"
    "ds_read_b64_tr_b16 v[60:61], v8 offset:20992\n"
    "ds_read_b64_tr_b16 v[62:63], v8 offset:23040\n"
    "s_add_u32 m0, s67, 3072\n"
    "s_lshl3_add_u32 s79, s32, 24576\n"
    "buffer_load_dwordx4 v6, s[24:27], s79 offen offset:0 lds\n"
    "s_add_u32 m0, s67, 4096\n"
    "s_lshl3_add_u32 s79, s32, 32768\n"
    "buffer_load_dwordx4 v6, s[24:27], s79 offen offset:0 lds\n"
    "s_add_u32 m0, s67, 5120\n"
    "s_lshl3_add_u32 s79, s32, 40960\n"
    "buffer_load_dwordx4 v6, s[24:27], s79 offen offset:0 lds\n"
    "s_add_u32 m0, s67, 6144\n"
    "s_lshl3_add_u32 s79, s32, 49152\n"
    "buffer_load_dwordx4 v6, s[24:27], s79 offen offset:0 lds\n"
    "s_add_u32 m0, s67, 7168\n"
    "s_lshl3_add_u32 s79, s32, 57344\n"
    "buffer_load_dwordx4 v6, s[24:27], s79 offen offset:0 lds\n"
    "s_waitcnt lgkmcnt(0)\n"
    "s_add_u32 m0, s67, 16384\n"
    "s_lshl3_add_u32 s79, s33, 0\n"
    "buffer_load_dwordx4 v6, s[24:27], s79 offen offset:0 lds\n"
    "s_add_u32 m0, s67, 17408\n"
    "s_lshl3_add_u32 s79, s33, 8192\n"
    "buffer_load_dwordx4 v6, s[24:27], s79 offen offset:0 lds\n"
    "s_waitcnt vmcnt(10)\n"
    "ds_read_b64_tr_b16 v[64:65], v7 offset:32768\n"
    "ds_read_b64_tr_b16 v[66:67], v7 offset:34816\n"
    "ds_read_b64_tr_b16 v[68:69], v8 offset:32768\n"
    "ds_read_b64_tr_b16 v[70:71], v8 offset:34816\n"
    "ds_read_b64_tr_b16 v[72:73], v7 offset:33280\n"
    "ds_read_b64_tr_b16 v[74:75], v7 offset:35328\n"
    "ds_read_b64_tr_b16 v[76:77], v8 offset:33280\n"
    "ds_read_b64_tr_b16 v[78:79], v8 offset:35328\n"
    "ds_read_b64_tr_b16 v[80:81], v7 offset:36864\n"
    "ds_read_b64_tr_b16 v[82:83], v7 offset:38912\n"
    "ds_read_b64_tr_b16 v[84:85], v8 offset:36864\n"
    "ds_read_b64_tr_b16 v[86:87], v8 offset:38912\n"
    "ds_read_b64_tr_b16 v[88:89], v7 offset:37376\n"
    "ds_read_b64_tr_b16 v[90:91], v7 offset:39424\n"
    "ds_read_b64_tr_b16 v[92:93], v8 offset:37376\n"
    "ds_read_b64_tr_b16 v[94:95], v8 offset:39424\n"
    "s_add_u32 m0, s67, 18432\n"
    "s_lshl3_add_u32 s79, s33, 16384\n"
    "buffer_load_dwordx4 v6, s[24:27], s79 offen offset:0 lds\n"
    "s_add_u32 m0, s67, 19456\n"
    "s_lshl3_add_u32 s79, s33, 24576\n"
    "buffer_load_dwordx4 v6, s[24:27], s79 offen offset:0 lds\n"
    "s_add_u32 m0, s67, 20480\n"
    "s_lshl3_add_u32 s79, s33, 32768\n"
    "buffer_load_dwordx4 v6, s[24:27], s79 offen offset:0 lds\n"
    "s_add_u32 m0, s67, 21504\n"
    "s_lshl3_add_u32 s79, s33, 40960\n"
    "buffer_load_dwordx4 v6, s[24:27], s79 offen offset:0 lds\n"
    "s_add_u32 m0, s67, 22528\n"
    "s_lshl3_add_u32 s79, s33, 49152\n"
    "buffer_load_dwordx4 v6, s[24:27], s79 offen offset:0 lds\n"
    "s_add_u32 m0, s67, 23552\n"
    "s_lshl3_add_u32 s79, s33, 57344\n"
    "buffer_load_dwordx4 v6, s[24:27], s79 offen offset:0 lds\n"
    "s_waitcnt lgkmcnt(0)\n"
    "s_add_u32 m0, s67, 32768\n"
    "s_lshl3_add_u32 s79, s34, 0\n"
    "buffer_load_dwordx4 v6, s[24:27], s79 offen offset:0 lds\n"
    "s_mov_b32 s70, s73\n"
    "s_add_u32 s71, s67, 32768\n"
    "s_add_u32 m0, s67, 33792\n"
    "s_lshl3_add_u32 s79, s34, 8192\n"
    "buffer_load_dwordx4 v6, s[24:27], s79 offen offset:0 lds\n"
    "s_lshl_b32 s72, s63, 13\n"
    "s_add_u32 s72, s72, 98304\n"
    "s_add_u32 m0, s67, 34816\n"
    "s_lshl3_add_u32 s79, s34, 16384\n"
    "buffer_load_dwordx4 v6, s[24:27], s79 offen offset:0 lds\n"
    "v_xor_b32 v21, v16, v5\n"
    "v_lshlrev_b32 v21, 4, v21\n"
    "s_add_u32 m0, s67, 35840\n"
    "s_lshl3_add_u32 s79, s34, 24576\n"
    "buffer_load_dwordx4 v6, s[24:27], s79 offen offset:0 lds\n"
    "v_lshl_add_u32 v21, v4, 8, v21\n"
    "v_add_u32 v11, s71, v21\n"
    "s_add_u32 m0, s67, 36864\n"
    "s_lshl3_add_u32 s79, s34, 32768\n"
    "buffer_load_dwordx4 v6, s[24:27], s79 offen offset:0 lds\n"
    "v_add_u32 v13, s72, v21\n"
    "v_xor_b32 v12, 64, v11\n"
    "s_add_u32 m0, s67, 37888\n"
    "s_lshl3_add_u32 s79, s34, 40960\n"
    "buffer_load_dwordx4 v6, s[24:27], s79 offen offset:0 lds\n"
    "v_xor_b32 v14, 64, v13\n"
    "s_add_u32 m0, s67, 38912\n"
    "s_lshl3_add_u32 s79, s34, 49152\n"
    "buffer_load_dwordx4 v6, s[24:27], s79 offen offset:0 lds\n"
    "s_add_u32 m0, s67, 39936\n"
    "s_lshl3_add_u32 s79, s34, 57344\n"
    "buffer_load_dwordx4 v6, s[24:27], s79 offen offset:0 lds\n"
    "buffer_load_dwordx2 v[22:23], v15, s[16:19], 0 offen offset:128 sc1\n"
    "s_waitcnt vmcnt(0)\n"
    "v_cmp_ne_u64 vcc, s[52:53], v[22:23]\n"
    "s_cbranch_vccnz Lsp5_%=\n"
    "Lsb5_%=:\n"
    "ds_write_b32 v27, v28 offset:32\n"
    "s_setprio 0\n"
    "s_add_u32 s79, s70, s28\n"
    "s_add_u32 s79, s79, 0\n"
    "buffer_load_dwordx4 v[160:163], v2, s[16:19], s79 offen offset:0 sc1\n"
    "buffer_load_dwordx4 v[164:167], v2, s[16:19], s79 offen offset:1024 sc1\n"
    "buffer_load_dwordx4 v[168:171], v2, s[16:19], s79 offen offset:2048 sc1\n"
    "buffer_load_dwordx4 v[172:175], v2, s[16:19], s79 offen offset:3072 sc1\n"
    "s_add_u32 s79, s70, s28\n"
    "s_add_u32 s79, s79, 4096\n"
    "buffer_load_dwordx4 v[176:179], v2, s[16:19], s79 offen offset:0 sc1\n"
    "buffer_load_dwordx4 v[180:183], v2, s[16:19], s79 offen offset:1024 sc1\n"
    "buffer_load_dwordx4 v[184:187], v2, s[16:19], s79 offen offset:2048 sc1\n"
    "buffer_load_dwordx4 v[188:191], v2, s[16:19], s79 offen offset:3072 sc1\n"
    "s_waitcnt vmcnt(7)\n"
    "v_mfma_f32_16x16x32_bf16 v[96:99], v[192:195], v[160:163], 0\n"
    "v_mfma_f32_16x16x32_bf16 v[112:115], v[196:199], v[160:163], 0\n"
    "v_mfma_f32_16x16x32_bf16 v[128:131], v[200:203], v[160:163], 0\n"
    "v_mfma_f32_16x16x32_bf16 v[144:147], v[204:207], v[160:163], 0\n"
    "s_waitcnt vmcnt(6)\n"
    "v_mfma_f32_16x16x32_bf16 v[100:103], v[192:195], v[164:167], 0\n"
    "v_mfma_f32_16x16x32_bf16 v[116:119], v[196:199], v[164:167], 0\n"
    "v_mfma_f32_16x16x32_bf16 v[132:135], v[200:203], v[164:167], 0\n"
    "v_mfma_f32_16x16x32_bf16 v[148:151], v[204:207], v[164:167], 0\n"
    "s_waitcnt vmcnt(5)\n"
    "v_mfma_f32_16x16x32_bf16 v[104:107], v[192:195], v[168:171], 0\n"
    "v_mfma_f32_16x16x32_bf16 v[120:123], v[196:199], v[168:171], 0\n"
    "v_mfma_f32_16x16x32_bf16 v[136:139], v[200:203], v[168:171], 0\n"
    "v_mfma_f32_16x16x32_bf16 v[152:155], v[204:207], v[168:171], 0\n"
    "s_waitcnt vmcnt(4)\n"
    "v_mfma_f32_16x16x32_bf16 v[108:111], v[192:195], v[172:175], 0\n"
    "v_mfma_f32_16x16x32_bf16 v[124:127], v[196:199], v[172:175], 0\n"
    "v_mfma_f32_16x16x32_bf16 v[140:143], v[200:203], v[172:175], 0\n"
    "v_mfma_f32_16x16x32_bf16 v[156:159], v[204:207], v[172:175], 0\n"
    "s_waitcnt vmcnt(3)\n"
    "v_mfma_f32_16x16x32_bf16 v[96:99], v[208:211], v[176:179], v[96:99]\n"
    "s_add_u32 s79, s70, s29\n"
    "s_add_u32 s79, s79, 0\n"
    "buffer_load_dwordx4 v[160:163], v2, s[16:19], s79 offen offset:0 sc1\n"
    "v_mfma_f32_16x16x32_bf16 v[112:115], v[212:215], v[176:179], v[112:115]\n"
    "v_mfma_f32_16x16x32_bf16 v[128:131], v[216:219], v[176:179], v[128:131]\n"
    "buffer_load_dwordx4 v[164:167], v2, s[16:19], s79 offen offset:1024 sc1\n"
    "v_mfma_f32_16x16x32_bf16 v[144:147], v[220:223], v[176:179], v[144:147]\n"
    "s_waitcnt vmcnt(4)\n"
    "v_mfma_f32_16x16x32_bf16 v[100:103], v[208:211], v[180:183], v[100:103]\n"
    "buffer_load_dwordx4 v[168:171], v2, s[16:19], s79 offen offset:2048 sc1\n"
    "v_mfma_f32_16x16x32_bf16 v[116:119], v[212:215], v[180:183], v[116:119]\n"
    "v_mfma_f32_16x16x32_bf16 v[132:135], v[216:219], v[180:183], v[132:135]\n"
    "buffer_load_dwordx4 v[172:175], v2, s[16:19], s79 offen offset:3072 sc1\n"
    "v_mfma_f32_16x16x32_bf16 v[148:151], v[220:223], v[180:183], v[148:151]\n"
    "s_waitcnt vmcnt(5)\n"
    "v_mfma_f32_16x16x32_bf16 v[104:107], v[208:211], v[184:187], v[104:107]\n"
    "s_add_u32 s79, s70, s29\n"
    "s_add_u32 s79, s79, 4096\n"
    "buffer_load_dwordx4 v[192:195], v2, s[16:19], s79 offen offset:0 sc1\n"
    "v_mfma_f32_16x16x32_bf16 v[120:123], v[212:215], v[184:187], v[120:123]\n"
    "v_mfma_f32_16x16x32_bf16 v[136:139], v[216:219], v[184:187], v[136:139]\n"
    "buffer_load_dwordx4 v[196:199], v2, s[16:19], s79 offen offset:1024 sc1\n"
    "v_mfma_f32_16x16x32_bf16 v[152:155], v[220:223], v[184:187], v[152:155]\n"
    "s_waitcnt vmcnt(6)\n"
    "v_mfma_f32_16x16x32_bf16 v[108:111], v[208:211], v[188:191], v[108:111]\n"
    "buffer_load_dwordx4 v[200:203], v2, s[16:19], s79 offen offset:2048 sc1\n"
    "v_mfma_f32_16x16x32_bf16 v[124:127], v[212:215], v[188:191], v[124:127]\n"
    "v_mfma_f32_16x16x32_bf16 v[140:143], v[216:219], v[188:191], v[140:143]\n"
    "buffer_load_dwordx4 v[204:207], v2, s[16:19], s79 offen offset:3072 sc1\n"
    "v_mfma_f32_16x16x32_bf16 v[156:159], v[220:223], v[188:191], v[156:159]\n"
    "s_waitcnt vmcnt(7)\n"
    "v_mfma_f32_16x16x32_bf16 v[96:99], v[224:227], v[160:163], v[96:99]\n"
    "s_add_u32 s79, s70, s30\n"
    "s_add_u32 s79, s79, 0\n"
    "buffer_load_dwordx4 v[176:179], v2, s[16:19], s79 offen offset:0 sc1\n"
    "v_mfma_f32_16x16x32_bf16 v[112:115], v[228:231], v[160:163], v[112:115]\n"
    "v_mfma_f32_16x16x32_bf16 v[128:131], v[232:235], v[160:163], v[128:131]\n"
    "v_mfma_f32_16x16x32_bf16 v[144:147], v[236:239], v[160:163], v[144:147]\n"
    "s_waitcnt vmcnt(7)\n"
    "v_mfma_f32_16x16x32_bf16 v[100:103], v[224:227], v[164:167], v[100:103]\n"
    "buffer_load_dwordx4 v[180:183], v2, s[16:19], s79 offen offset:1024 sc1\n"
    "v_mfma_f32_16x16x32_bf16 v[116:119], v[228:231], v[164:167], v[116:119]\n"
    "v_mfma_f32_16x16x32_bf16 v[132:135], v[232:235], v[164:167], v[132:135]\n"
    "v_mfma_f32_16x16x32_bf16 v[148:151], v[236:239], v[164:167], v[148:151]\n"
    "s_waitcnt vmcnt(7)\n"
    "v_mfma_f32_16x16x32_bf16 v[104:107], v[224:227], v[168:171], v[104:107]\n"
    "buffer_load_dwordx4 v[184:187], v2, s[16:19], s79 offen offset:2048 sc1\n"
    "v_mfma_f32_16x16x32_bf16 v[120:123], v[228:231], v[168:171], v[120:123]\n"
    "v_mfma_f32_16x16x32_bf16 v[136:139], v[232:235], v[168:171], v[136:139]\n"
    "v_mfma_f32_16x16x32_bf16 v[152:155], v[236:239], v[168:171], v[152:155]\n"
    "s_waitcnt vmcnt(7)\n"
    "v_mfma_f32_16x16x32_bf16 v[108:111], v[224:227], v[172:175], v[108:111]\n"
    "buffer_load_dwordx4 v[188:191], v2, s[16:19], s79 offen offset:3072 sc1\n"
    "v_mfma_f32_16x16x32_bf16 v[124:127], v[228:231], v[172:175], v[124:127]\n"
    "v_mfma_f32_16x16x32_bf16 v[140:143], v[232:235], v[172:175], v[140:143]\n"
    "v_mfma_f32_16x16x32_bf16 v[156:159], v[236:239], v[172:175], v[156:159]\n"
    "s_waitcnt vmcnt(7)\n"
    "v_mfma_f32_16x16x32_bf16 v[96:99], v[240:243], v[192:195], v[96:99]\n"
    "s_add_u32 s79, s70, s30\n"
    "s_add_u32 s79, s79, 4096\n"
    "buffer_load_dwordx4 v[208:211], v2, s[16:19], s79 offen offset:0 sc1\n"
    "v_mfma_f32_16x16x32_bf16 v[112:115], v[244:247], v[192:195], v[112:115]\n"
    "v_mfma_f32_16x16x32_bf16 v[128:131], v[248:251], v[192:195], v[128:131]\n"
    "buffer_load_dwordx4 v[212:215], v2, s[16:19], s79 offen offset:1024 sc1\n"
    "v_mfma_f32_16x16x32_bf16 v[144:147], v[252:255], v[192:195], v[144:147]\n"
    "s_waitcnt vmcnt(8)\n"
    "v_mfma_f32_16x16x32_bf16 v[100:103], v[240:243], v[196:199], v[100:103]\n"
    "buffer_load_dwordx4 v[216:219], v2, s[16:19], s79 offen offset:2048 sc1\n"
    "v_mfma_f32_16x16x32_bf16 v[116:119], v[244:247], v[196:199], v[116:119]\n"
    "v_mfma_f32_16x16x32_bf16 v[132:135], v[248:251], v[196:199], v[132:135]\n"
    "buffer_load_dwordx4 v[220:223], v2, s[16:19], s79 offen offset:3072 sc1\n"
    "v_mfma_f32_16x16x32_bf16 v[148:151], v[252:255], v[196:199], v[148:151]\n"
    "s_waitcnt vmcnt(9)\n"
    "v_mfma_f32_16x16x32_bf16 v[104:107], v[240:243], v[200:203], v[104:107]\n"
    "s_add_u32 s79, s70, s31\n"
    "s_add_u32 s79, s79, 0\n"
    "buffer_load_dwordx4 v[160:163], v2, s[16:19], s79 offen offset:0 sc1\n"
    "v_mfma_f32_16x16x32_bf16 v[120:123], v[244:247], v[200:203], v[120:123]\n"
    "v_mfma_f32_16x16x32_bf16 v[136:139], v[248:251], v[200:203], v[136:139]\n"
    "buffer_load_dwordx4 v[164:167], v2, s[16:19], s79 offen offset:1024 sc1\n"
    "v_mfma_f32_16x16x32_bf16 v[152:155], v[252:255], v[200:203], v[152:155]\n"
    "s_waitcnt vmcnt(10)\n"
    "v_mfma_f32_16x16x32_bf16 v[108:111], v[240:243], v[204:207], v[108:111]\n"
    "buffer_load_dwordx4 v[168:171], v2, s[16:19], s79 offen offset:2048 sc1\n"
    "v_mfma_f32_16x16x32_bf16 v[124:127], v[244:247], v[204:207], v[124:127]\n"
    "v_mfma_f32_16x16x32_bf16 v[140:143], v[248:251], v[204:207], v[140:143]\n"
    "buffer_load_dwordx4 v[172:175], v2, s[16:19], s79 offen offset:3072 sc1\n"
    "v_mfma_f32_16x16x32_bf16 v[156:159], v[252:255], v[204:207], v[156:159]\n"
    "s_waitcnt vmcnt(11)\n"
    "v_mfma_f32_16x16x32_bf16 v[96:99], v[32:35], v[176:179], v[96:99]\n"
    "s_add_u32 s79, s70, s31\n"
    "s_add_u32 s79, s79, 4096\n"
    "buffer_load_dwordx4 v[224:227], v2, s[16:19], s79 offen offset:0 sc1\n"
    "v_mfma_f32_16x16x32_bf16 v[112:115], v[36:39], v[176:179], v[112:115]\n"
    "v_mfma_f32_16x16x32_bf16 v[128:131], v[40:43], v[176:179], v[128:131]\n"
    "v_mfma_f32_16x16x32_bf16 v[144:147], v[44:47], v[176:179], v[144:147]\n"
    "s_waitcnt vmcnt(11)\n"
    "v_mfma_f32_16x16x32_bf16 v[100:103], v[32:35], v[180:183], v[100:103]\n"
    "buffer_load_dwordx4 v[228:231], v2, s[16:19], s79 offen offset:1024 sc1\n"
    "v_mfma_f32_16x16x32_bf16 v[116:119], v[36:39], v[180:183], v[116:119]\n"
    "v_mfma_f32_16x16x32_bf16 v[132:135], v[40:43], v[180:183], v[132:135]\n"
    "v_mfma_f32_16x16x32_bf16 v[148:151], v[44:47], v[180:183], v[148:151]\n"
    "s_waitcnt vmcnt(11)\n"
    "v_mfma_f32_16x16x32_bf16 v[104:107], v[32:35], v[184:187], v[104:107]\n"
    "buffer_load_dwordx4 v[232:235], v2, s[16:19], s79 offen offset:2048 sc1\n"
    "v_mfma_f32_16x16x32_bf16 v[120:123], v[36:39], v[184:187], v[120:123]\n"
    "v_mfma_f32_16x16x32_bf16 v[136:139], v[40:43], v[184:187], v[136:139]\n"
    "v_mfma_f32_16x16x32_bf16 v[152:155], v[44:47], v[184:187], v[152:155]\n"
    "s_waitcnt vmcnt(11)\n"
    "v_mfma_f32_16x16x32_bf16 v[108:111], v[32:35], v[188:191], v[108:111]\n"
    "buffer_load_dwordx4 v[236:239], v2, s[16:19], s79 offen offset:3072 sc1\n"
    "v_mfma_f32_16x16x32_bf16 v[124:127], v[36:39], v[188:191], v[124:127]\n"
    "v_mfma_f32_16x16x32_bf16 v[140:143], v[40:43], v[188:191], v[140:143]\n"
    "v_mfma_f32_16x16x32_bf16 v[156:159], v[44:47], v[188:191], v[156:159]\n"
    "s_waitcnt vmcnt(11)\n"
    "v_mfma_f32_16x16x32_bf16 v[96:99], v[48:51], v[208:211], v[96:99]\n"
    "s_add_u32 s79, s70, s34\n"
    "s_add_u32 s79, s79, 0\n"
    "buffer_load_dwordx4 v[192:195], v2, s[16:19], s79 offen offset:0 sc1\n"
    "v_mfma_f32_16x16x32_bf16 v[112:115], v[52:55], v[208:211], v[112:115]\n"
    "v_mfma_f32_16x16x32_bf16 v[128:131], v[56:59], v[208:211], v[128:131]\n"
    "v_mfma_f32_16x16x32_bf16 v[144:147], v[60:63], v[208:211], v[144:147]\n"
    "s_waitcnt vmcnt(11)\n"
    "v_mfma_f32_16x16x32_bf16 v[100:103], v[48:51], v[212:215], v[100:103]\n"
    "buffer_load_dwordx4 v[196:199], v2, s[16:19], s79 offen offset:1024 sc1\n"
    "v_mfma_f32_16x16x32_bf16 v[116:119], v[52:55], v[212:215], v[116:119]\n"
    "v_mfma_f32_16x16x32_bf16 v[132:135], v[56:59], v[212:215], v[132:135]\n"
    "v_mfma_f32_16x16x32_bf16 v[148:151], v[60:63], v[212:215], v[148:151]\n"
    "s_waitcnt vmcnt(11)\n"
    "v_mfma_f32_16x16x32_bf16 v[104:107], v[48:51], v[216:219], v[104:107]\n"
    "buffer_load_dwordx4 v[200:203], v2, s[16:19], s79 offen offset:2048 sc1\n"
    "v_mfma_f32_16x16x32_bf16 v[120:123], v[52:55], v[216:219], v[120:123]\n"
    "v_mfma_f32_16x16x32_bf16 v[136:139], v[56:59], v[216:219], v[136:139]\n"
    "v_mfma_f32_16x16x32_bf16 v[152:155], v[60:63], v[216:219], v[152:155]\n"
    "s_waitcnt vmcnt(11)\n"
    "v_mfma_f32_16x16x32_bf16 v[108:111], v[48:51], v[220:223], v[108:111]\n"
    "buffer_load_dwordx4 v[204:207], v2, s[16:19], s79 offen offset:3072 sc1\n"
    "v_mfma_f32_16x16x32_bf16 v[124:127], v[52:55], v[220:223], v[124:127]\n"
    "v_mfma_f32_16x16x32_bf16 v[140:143], v[56:59], v[220:223], v[140:143]\n"
    "v_mfma_f32_16x16x32_bf16 v[156:159], v[60:63], v[220:223], v[156:159]\n"
    "s_waitcnt vmcnt(11)\n"
    "v_mfma_f32_16x16x32_bf16 v[96:99], v[64:67], v[160:163], v[96:99]\n"
    "s_add_u32 s79, s70, s34\n"
    "s_add_u32 s79, s79, 4096\n"
    "buffer_load_dwordx4 v[240:243], v2, s[16:19], s79 offen offset:0 sc1\n"
    "ds_read_b64_tr_b16 v[176:177], v7 offset:32768\n"
    "v_mfma_f32_16x16x32_bf16 v[112:115], v[68:71], v[160:163], v[112:115]\n"
    "ds_read_b64_tr_b16 v[178:179], v7 offset:34816\n"
    "v_mfma_f32_16x16x32_bf16 v[128:131], v[72:75], v[160:163], v[128:131]\n"
    "ds_read_b64_tr_b16 v[180:181], v8 offset:32768\n"
    "v_mfma_f32_16x16x32_bf16 v[144:147], v[76:79], v[160:163], v[144:147]\n"
    "ds_read_b64_tr_b16 v[182:183], v8 offset:34816\n"
    "s_waitcnt vmcnt(11)\n"
    "v_mfma_f32_16x16x32_bf16 v[100:103], v[64:67], v[164:167], v[100:103]\n"
    "ds_read_b64_tr_b16 v[184:185], v7 offset:33280\n"
    "buffer_load_dwordx4 v[244:247], v2, s[16:19], s79 offen offset:1024 sc1\n"
    "ds_read_b64_tr_b16 v[186:187], v7 offset:35328\n"
    "v_mfma_f32_16x16x32_bf16 v[116:119], v[68:71], v[164:167], v[116:119]\n"
    "ds_read_b64_tr_b16 v[188:189], v8 offset:33280\n"
    "v_mfma_f32_16x16x32_bf16 v[132:135], v[72:75], v[164:167], v[132:135]\n"
    "ds_read_b64_tr_b16 v[190:191], v8 offset:35328\n"
    "v_mfma_f32_16x16x32_bf16 v[148:151], v[76:79], v[164:167], v[148:151]\n"
    "s_waitcnt vmcnt(11)\n"
    "v_mfma_f32_16x16x32_bf16 v[104:107], v[64:67], v[168:171], v[104:107]\n"
    "buffer_load_dwordx4 v[248:251], v2, s[16:19], s79 offen offset:2048 sc1\n"
    "v_mfma_f32_16x16x32_bf16 v[120:123], v[68:71], v[168:171], v[120:123]\n"
    "v_mfma_f32_16x16x32_bf16 v[136:139], v[72:75], v[168:171], v[136:139]\n"
    "v_mfma_f32_16x16x32_bf16 v[152:155], v[76:79], v[168:171], v[152:155]\n"
    "s_waitcnt vmcnt(11)\n"
    "v_mfma_f32_16x16x32_bf16 v[108:111], v[64:67], v[172:175], v[108:111]\n"
    "buffer_load_dwordx4 v[252:255], v2, s[16:19], s79 offen offset:3072 sc1\n"
    "v_mfma_f32_16x16x32_bf16 v[124:127], v[68:71], v[172:175], v[124:127]\n"
    "v_mfma_f32_16x16x32_bf16 v[140:143], v[72:75], v[172:175], v[140:143]\n"
    "v_mfma_f32_16x16x32_bf16 v[156:159], v[76:79], v[172:175], v[156:159]\n"
    "s_waitcnt vmcnt(11)\n"
    "v_mfma_f32_16x16x32_bf16 v[96:99], v[80:83], v[224:227], v[96:99]\n"
    "v_mfma_f32_16x16x32_bf16 v[112:115], v[84:87], v[224:227], v[112:115]\n"
    "ds_read_b64_tr_b16 v[32:33], v7 offset:36864\n"
    "v_mfma_f32_16x16x32_bf16 v[128:131], v[88:91], v[224:227], v[128:131]\n"
    "ds_read_b64_tr_b16 v[34:35], v7 offset:38912\n"
    "v_mfma_f32_16x16x32_bf16 v[144:147], v[92:95], v[224:227], v[144:147]\n"
    "ds_read_b64_tr_b16 v[36:37], v8 offset:36864\n"
    "s_waitcnt vmcnt(10)\n"
    "v_mfma_f32_16x16x32_bf16 v[100:103], v[80:83], v[228:231], v[100:103]\n"
    "ds_read_b64_tr_b16 v[38:39], v8 offset:38912\n"
    "v_mfma_f32_16x16x32_bf16 v[116:119], v[84:87], v[228:231], v[116:119]\n"
    "ds_read_b64_tr_b16 v[40:41], v7 offset:37376\n"
    "v_mfma_f32_16x16x32_bf16 v[132:135], v[88:91], v[228:231], v[132:135]\n"
    "ds_read_b64_tr_b16 v[42:43], v7 offset:39424\n"
    "v_mfma_f32_16x16x32_bf16 v[148:151], v[92:95], v[228:231], v[148:151]\n"
    "ds_read_b64_tr_b16 v[44:45], v8 offset:37376\n"
    "s_waitcnt vmcnt(9)\n"
    "v_mfma_f32_16x16x32_bf16 v[104:107], v[80:83], v[232:235], v[104:107]\n"
    "ds_read_b64_tr_b16 v[46:47], v8 offset:39424\n"
    "v_mfma_f32_16x16x32_bf16 v[120:123], v[84:87], v[232:235], v[120:123]\n"
    "v_mfma_f32_16x16x32_bf16 v[136:139], v[88:91], v[232:235], v[136:139]\n"
    "v_mfma_f32_16x16x32_bf16 v[152:155], v[92:95], v[232:235], v[152:155]\n"
    "s_waitcnt vmcnt(8)\n"
    "v_mfma_f32_16x16x32_bf16 v[108:111], v[80:83], v[236:239], v[108:111]\n"
    "v_mfma_f32_16x16x32_bf16 v[124:127], v[84:87], v[236:239], v[124:127]\n"
    "v_mfma_f32_16x16x32_bf16 v[140:143], v[88:91], v[236:239], v[140:143]\n"
    "v_mfma_f32_16x16x32_bf16 v[156:159], v[92:95], v[236:239], v[156:159]\n"
    "s_waitcnt vmcnt(7) lgkmcnt(8)\n"
    "v_mfma_f32_16x16x32_bf16 v[96:99], v[176:179], v[192:195], v[96:99]\n"
    "v_mfma_f32_16x16x32_bf16 v[112:115], v[180:183], v[192:195], v[112:115]\n"
    "ds_read_b32 v26, v31 offset:48\n"
    "v_mfma_f32_16x16x32_bf16 v[128:131], v[184:187], v[192:195], v[128:131]\n"
    "v_mfma_f32_16x16x32_bf16 v[144:147], v[188:191], v[192:195], v[144:147]\n"
    "s_waitcnt vmcnt(6)\n"
    "v_mfma_f32_16x16x32_bf16 v[100:103], v[176:179], v[196:199], v[100:103]\n"
    "v_mfma_f32_16x16x32_bf16 v[116:119], v[180:183], v[196:199], v[116:119]\n"
    "v_mfma_f32_16x16x32_bf16 v[132:135], v[184:187], v[196:199], v[132:135]\n"
    "v_mfma_f32_16x16x32_bf16 v[148:151], v[188:191], v[196:199], v[148:151]\n"
    "s_waitcnt vmcnt(5)\n"
    "v_mfma_f32_16x16x32_bf16 v[104:107], v[176:179], v[200:203], v[104:107]\n"
    "v_mfma_f32_16x16x32_bf16 v[120:123], v[180:183], v[200:203], v[120:123]\n"
    "v_mfma_f32_16x16x32_bf16 v[136:139], v[184:187], v[200:203], v[136:139]\n"
    "v_mfma_f32_16x16x32_bf16 v[152:155], v[188:191], v[200:203], v[152:155]\n"
    "s_waitcnt vmcnt(4)\n"
    "v_mfma_f32_16x16x32_bf16 v[108:111], v[176:179], v[204:207], v[108:111]\n"
    "v_mfma_f32_16x16x32_bf16 v[124:127], v[180:183], v[204:207], v[124:127]\n"
    "v_mfma_f32_16x16x32_bf16 v[140:143], v[184:187], v[204:207], v[140:143]\n"
    "v_mfma_f32_16x16x32_bf16 v[156:159], v[188:191], v[204:207], v[156:159]\n"
    "s_waitcnt lgkmcnt(0)\n"
    "v_cmp_ne_u32 vcc, 1, v26\n"
    "s_cbranch_vccnz Lsp6_%=\n"
    "Lsb6_%=:\n"
    "s_waitcnt vmcnt(3)\n"
    "v_mfma_f32_16x16x32_bf16 v[96:99], v[32:35], v[240:243], v[96:99]\n"
    "v_mfma_f32_16x16x32_bf16 v[112:115], v[36:39], v[240:243], v[112:115]\n"
    "v_mfma_f32_16x16x32_bf16 v[128:131], v[40:43], v[240:243], v[128:131]\n"
    "v_mfma_f32_16x16x32_bf16 v[144:147], v[44:47], v[240:243], v[144:147]\n"
    "s_waitcnt vmcnt(2)\n"
    "v_mfma_f32_16x16x32_bf16 v[100:103], v[32:35], v[244:247], v[100:103]\n"
    "v_mfma_f32_16x16x32_bf16 v[116:119], v[36:39], v[244:247], v[116:119]\n"
    "v_mfma_f32_16x16x32_bf16 v[132:135], v[40:43], v[244:247], v[132:135]\n"
    "s_nop 0\n"
    "ds_write_b128 v11, v[96:99] offset:0\n"
    "v_mfma_f32_16x16x32_bf16 v[148:151], v[44:47], v[244:247], v[148:151]\n"
    "ds_write_b128 v12, v[112:115] offset:0\n"
    "s_waitcnt vmcnt(1)\n"
    "v_mfma_f32_16x16x32_bf16 v[104:107], v[32:35], v[248:251], v[104:107]\n"
    "ds_write_b128 v11, v[128:131] offset:128\n"
    "v_mfma_f32_16x16x32_bf16 v[120:123], v[36:39], v[248:251], v[120:123]\n"
    "ds_write_b128 v12, v[144:147] offset:128\n"
    "v_mfma_f32_16x16x32_bf16 v[136:139], v[40:43], v[248:251], v[136:139]\n"
    "ds_write_b128 v11, v[100:103] offset:4096\n"
    "v_mfma_f32_16x16x32_bf16 v[152:155], v[44:47], v[248:251], v[152:155]\n"
    "ds_write_b128 v12, v[116:119] offset:4096\n"
    "s_waitcnt vmcnt(0)\n"
    "v_mfma_f32_16x16x32_bf16 v[108:111], v[32:35], v[252:255], v[108:111]\n"
    "ds_write_b128 v11, v[132:135] offset:4224\n"
    "v_mfma_f32_16x16x32_bf16 v[124:127], v[36:39], v[252:255], v[124:127]\n"
    "ds_write_b128 v12, v[148:151] offset:4224\n"
    "v_mfma_f32_16x16x32_bf16 v[140:143], v[40:43], v[252:255], v[140:143]\n"
    "ds_write_b128 v13, v[104:107] offset:0\n"
    "v_mfma_f32_16x16x32_bf16 v[156:159], v[44:47], v[252:255], v[156:159]\n"
    "ds_write_b128 v14, v[120:123] offset:0\n"
    "ds_write_b128 v13, v[136:139] offset:128\n"
    "ds_write_b128 v14, v[152:155] offset:128\n"
    "ds_write_b128 v13, v[108:111] offset:4096\n"
    "ds_write_b128 v14, v[124:127] offset:4096\n"
    "s_nop 0\n"
    "ds_write_b128 v13, v[140:143] offset:4224\n"
    "s_nop 0\n"
    "ds_write_b128 v14, v[156:159] offset:4224\n"
    "s_waitcnt lgkmcnt(0)\n"
    "s_barrier\n"
    "s_endpgm\n"
    "Lcomp_%=:\n"
    "v_lshrrev_b32 v21, 3, v4\n"
    "v_lshl_add_u32 v21, v16, 1, v21\n"
    "s_lshl_b32 s78, s63, 5\n"
    "v_and_b32 v22, 7, v21\n"
    "v_lshrrev_b32 v23, 1, v5\n"
    "v_xor_b32 v22, v22, v23\n"
    "v_lshlrev_b32 v22, 4, v22\n"
    "v_xor_b32 v22, s78, v22\n"
    "v_lshl_add_u32 v22, v21, 9, v22\n"
    "v_and_b32 v23, 1, v5\n"
    "v_lshl_add_u32 v22, v23, 3, v22\n"
    "v_add_u32 v232, 140800, v22\n"
    "v_lshlrev_b32 v21, 2, v21\n"
    "v_sub_u32 v21, v21, v5\n"
    "v_add_u32 v22, 8, v21\n"
    "v_bfe_u32 v23, v22, 2, 3\n"
    "v_lshlrev_b32 v23, 4, v23\n"
    "v_xor_b32 v23, s78, v23\n"
    "v_lshl_add_u32 v22, v22, 7, v23\n"
    "v_add_u32 v224, 131584, v22\n"
    "v_xor_b32 v225, 16, v224\n"
    "v_add_u32 v22, 9, v21\n"
    "v_bfe_u32 v23, v22, 2, 3\n"
    "v_lshlrev_b32 v23, 4, v23\n"
    "v_xor_b32 v23, s78, v23\n"
    "v_lshl_add_u32 v22, v22, 7, v23\n"
    "v_add_u32 v226, 131584, v22\n"
    "v_xor_b32 v227, 16, v226\n"
    "v_add_u32 v22, 10, v21\n"
    "v_bfe_u32 v23, v22, 2, 3\n"
    "v_lshlrev_b32 v23, 4, v23\n"
    "v_xor_b32 v23, s78, v23\n"
    "v_lshl_add_u32 v22, v22, 7, v23\n"
    "v_add_u32 v228, 131584, v22\n"
    "v_xor_b32 v229, 16, v228\n"
    "v_add_u32 v22, 11, v21\n"
    "v_bfe_u32 v23, v22, 2, 3\n"
    "v_lshlrev_b32 v23, 4, v23\n"
    "v_xor_b32 v23, s78, v23\n"
    "v_lshl_add_u32 v22, v22, 7, v23\n"
    "v_add_u32 v230, 131584, v22\n"
    "v_xor_b32 v231, 16, v230\n"
    "v_lshlrev_b32 v233, 4, v4\n"
    "v_lshl_add_u32 v233, v5, 9, v233\n"
    "s_lshl2_add_u32 s36, s55, s63\n"
    "s_lshl_b32 s36, s36, 11\n"
    "s_add_u32 s36, s36, 1048576\n"
    "s_and_b32 s77, s58, 7\n"
    "s_lshl2_add_u32 s77, s77, s63\n"
    "s_lshl_b32 s77, s77, 5\n"
    "s_lshr_b32 s78, s55, 3\n"
    "s_add_u32 s77, s77, s78\n"
    "s_lshl_b32 s77, s77, 7\n"
    "s_add_u32 s37, s77, 786432\n"
    "v_lshrrev_b32 v21, 1, v4\n"
    "v_xor_b32 v21, v21, v5\n"
    "v_lshlrev_b32 v21, 4, v21\n"
    "v_lshl_add_u32 v21, v4, 7, v21\n"
    "s_lshl_b32 s77, s65, 13\n"
    "v_add_u32 v11, s77, v21\n"
    "v_xor_b32 v12, 64, v11\n"
    "v_bfe_u32 v21, v0, 2, 2\n"
    "v_lshlrev_b32 v21, 6, v21\n"
    "v_lshl_add_u32 v21, v5, 11, v21\n"
    "v_and_b32 v23, 1, v5\n"
    "v_lshlrev_b32 v23, 5, v23\n"
    "v_and_b32 v22, 3, v0\n"
    "v_lshl_add_u32 v22, v22, 3, v23\n"
    "s_lshl_b32 s77, s64, 10\n"
    "s_add_u32 s77, s77, 49152\n"
    "v_add3_u32 v13, v21, v22, s77\n"
    "v_xor_b32 v14, 272, v13\n"
    "v_xor_b32 v15, 32, v13\n"
    "v_xor_b32 v30, 304, v13\n"
    "v_mov_b32 v92, 0\n"
    "v_mov_b32 v93, 0\n"
    "v_mov_b32 v94, 0\n"
    "v_mov_b32 v95, 0\n"
    "v_lshrrev_b32 v22, 2, v1\n"
    "v_mul_u32_u24 v22, 4096, v22\n"
    "s_mul_i32 s77, s63, 8320\n"
    "s_add_u32 s77, s77, 98304\n"
    "v_add3_u32 v22, v22, v2, s77\n"
    "v_cmp_gt_u32 vcc, 8, v1\n"
    "v_add_u32 v23, 153216, v2\n"
    "s_mul_i32 s77, s63, 4640\n"
    "v_cndmask_b32 v22, v23, v22, vcc\n"
    "s_add_u32 s77, s77, 131584\n"
    "v_add_u32 v239, s77, v2\n"
    "s_mov_b64 exec, 0xff77\n"
    "ds_write_b128 v22, v[92:95]\n"
    "s_mov_b64 exec, -1\n"
    "s_lshl1_add_u32 s77, s59, s65\n"
    "s_mul_i32 s77, s77, 28\n"
    "s_lshl1_add_u32 s78, s60, s64\n"
    "s_add_u32 s77, s77, s78\n"
    "s_lshl_b32 s69, s77, 8\n"
    "s_add_u32 s69, s69, 262144\n"
    "s_lshl_b32 s68, s77, 13\n"
    "s_add_u32 s68, s68, 5242880\n"
    "s_add_u32 s76, s68, 4096\n"
    "s_waitcnt lgkmcnt(0)\n"
    "s_add_u32 s52, s52, 0x7f4a7c15\n"
    "s_addc_u32 s53, s53, 0x9e3779b9\n"
    "v_mov_b32 v18, s52\n"
    "v_mov_b32 v19, s53\n"
    "s_lshl_b32 s54, s55, 2\n"
    "s_buffer_load_dword s54, s[40:43], s54\n"
    "s_waitcnt vmcnt(0)\n"
    "s_lshl_b32 s74, s73, 4\n"
    "s_add_u32 s74, s74, 131072\n"
    "v_lshlrev_b32 v52, 16, v32\n"
    "v_lshlrev_b32 v64, 16, v36\n"
    "v_and_b32 v53, 0xffff0000, v32\n"
    "v_and_b32 v65, 0xffff0000, v36\n"
    "v_lshlrev_b32 v54, 16, v33\n"
    "v_lshlrev_b32 v66, 16, v37\n"
    "v_and_b32 v55, 0xffff0000, v33\n"
    "v_and_b32 v67, 0xffff0000, v37\n"
    "v_lshlrev_b32 v56, 16, v34\n"
    "v_lshlrev_b32 v68, 16, v38\n"
    "v_and_b32 v57, 0xffff0000, v34\n"
    "v_and_b32 v69, 0xffff0000, v38\n"
    "v_lshlrev_b32 v58, 16, v35\n"
    "v_lshlrev_b32 v70, 16, v39\n"
    "v_and_b32 v59, 0xffff0000, v35\n"
    "v_and_b32 v71, 0xffff0000, v39\n"
    "v_pk_add_f32 v[60:61], v[52:53], v[54:55]\n"
    "v_pk_add_f32 v[72:73], v[64:65], v[66:67]\n"
    "v_pk_add_f32 v[62:63], v[56:57], v[58:59]\n"
    "v_pk_add_f32 v[74:75], v[68:69], v[70:71]\n"
    "v_pk_add_f32 v[60:61], v[60:61], v[62:63]\n"
    "v_pk_add_f32 v[72:73], v[72:73], v[74:75]\n"
    "v_add_f32 v60, v60, v61\n"
    "v_add_f32 v72, v72, v73\n"
    "s_nop 0\n"
    "v_add_f32_dpp v60, v60, v60 row_ror:8 row_mask:0xf bank_mask:0xf\n"
    "v_add_f32_dpp v72, v72, v72 row_ror:8 row_mask:0xf bank_mask:0xf\n"
    "s_nop 0\n"
    "v_add_f32_dpp v60, v60, v60 row_ror:4 row_mask:0xf bank_mask:0xf\n"
    "v_add_f32_dpp v72, v72, v72 row_ror:4 row_mask:0xf bank_mask:0xf\n"
    "s_nop 0\n"
    "v_add_f32_dpp v60, v60, v60 row_ror:2 row_mask:0xf bank_mask:0xf\n"
    "v_add_f32_dpp v72, v72, v72 row_ror:2 row_mask:0xf bank_mask:0xf\n"
    "s_nop 0\n"
    "v_add_f32_dpp v60, v60, v60 row_ror:1 row_mask:0xf bank_mask:0xf\n"
    "v_add_f32_dpp v72, v72, v72 row_ror:1 row_mask:0xf bank_mask:0xf\n"
    "v_mov_b32 v61, v60\n"
    "v_mov_b32 v73, v72\n"
    "s_nop 0\n"
    "v_permlane16_swap_b32 v60, v61\n"
    "v_permlane16_swap_b32 v72, v73\n"
    "v_add_f32 v60, v60, v61\n"
    "v_add_f32 v72, v72, v73\n"
    "v_mov_b32 v61, v60\n"
    "v_mov_b32 v73, v72\n"
    "s_nop 0\n"
    "v_permlane32_swap_b32 v60, v61\n"
    "v_permlane32_swap_b32 v72, v73\n"
    "v_add_f32 v60, v60, v61\n"
    "v_add_f32 v72, v72, v73\n"
    "v_mul_f32 v61, 0xbb000000, v60\n"
    "v_mul_f32 v73, 0xbb000000, v72\n"
    "v_mul_f32 v60, 0xbb000000, v60\n"
    "v_mul_f32 v72, 0xbb000000, v72\n"
    "v_pk_add_f32 v[52:53], v[52:53], v[60:61]\n"
    "v_pk_add_f32 v[64:65], v[64:65], v[72:73]\n"
    "v_pk_add_f32 v[54:55], v[54:55], v[60:61]\n"
    "v_pk_add_f32 v[66:67], v[66:67], v[72:73]\n"
    "v_pk_add_f32 v[56:57], v[56:57], v[60:61]\n"
    "v_pk_add_f32 v[68:69], v[68:69], v[72:73]\n"
    "v_pk_add_f32 v[58:59], v[58:59], v[60:61]\n"
    "v_pk_add_f32 v[70:71], v[70:71], v[72:73]\n"
    "v_pk_mul_f32 v[62:63], v[52:53], v[52:53]\n"
    "v_pk_mul_f32 v[74:75], v[64:65], v[64:65]\n"
    "v_pk_fma_f32 v[62:63], v[54:55], v[54:55], v[62:63]\n"
    "v_pk_fma_f32 v[74:75], v[66:67], v[66:67], v[74:75]\n"
    "v_pk_fma_f32 v[62:63], v[56:57], v[56:57], v[62:63]\n"
    "v_pk_fma_f32 v[74:75], v[68:69], v[68:69], v[74:75]\n"
    "v_pk_fma_f32 v[62:63], v[58:59], v[58:59], v[62:63]\n"
    "v_pk_fma_f32 v[74:75], v[70:71], v[70:71], v[74:75]\n"
    "v_add_f32 v62, v62, v63\n"
    "v_add_f32 v74, v74, v75\n"
    "s_nop 0\n"
    "v_add_f32_dpp v62, v62, v62 row_ror:8 row_mask:0xf bank_mask:0xf\n"
    "v_add_f32_dpp v74, v74, v74 row_ror:8 row_mask:0xf bank_mask:0xf\n"
    "s_nop 0\n"
    "v_add_f32_dpp v62, v62, v62 row_ror:4 row_mask:0xf bank_mask:0xf\n"
    "v_add_f32_dpp v74, v74, v74 row_ror:4 row_mask:0xf bank_mask:0xf\n"
    "s_nop 0\n"
    "v_add_f32_dpp v62, v62, v62 row_ror:2 row_mask:0xf bank_mask:0xf\n"
    "v_add_f32_dpp v74, v74, v74 row_ror:2 row_mask:0xf bank_mask:0xf\n"
    "s_nop 0\n"
    "v_add_f32_dpp v62, v62, v62 row_ror:1 row_mask:0xf bank_mask:0xf\n"
    "v_add_f32_dpp v74, v74, v74 row_ror:1 row_mask:0xf bank_mask:0xf\n"
    "v_mov_b32 v63, v62\n"
    "v_mov_b32 v75, v74\n"
    "s_nop 0\n"
    "v_permlane16_swap_b32 v62, v63\n"
    "v_permlane16_swap_b32 v74, v75\n"
    "v_add_f32 v62, v62, v63\n"
    "v_add_f32 v74, v74, v75\n"
    "v_mov_b32 v63, v62\n"
    "v_mov_b32 v75, v74\n"
    "s_nop 0\n"
    "v_permlane32_swap_b32 v62, v63\n"
    "v_permlane32_swap_b32 v74, v75\n"
    "v_add_f32 v62, v62, v63\n"
    "v_add_f32 v74, v74, v75\n"
    "v_mul_f32 v62, 0x3b000000, v62\n"
    "v_mul_f32 v74, 0x3b000000, v74\n"
    "v_mov_b32 v61, s52\n"
    "v_mov_b32 v73, s52\n"
    "v_mov_b32 v63, s53\n"
    "v_mov_b32 v75, s53\n"
    "buffer_store_dwordx4 v[60:63], v9, s[16:19], s74 offen offset:0 sc1\n"
    "buffer_store_dwordx4 v[72:75], v9, s[16:19], s74 offen offset:16 sc1\n"
    "v_and_b32 v23, 1, v0\n"
    "v_lshlrev_b32 v23, 4, v23\n"
    "v_lshlrev_b32 v234, v23, v234\n"
    "v_lshrrev_b32 v23, 1, v4\n"
    "v_cmp_eq_u32 vcc, 0, v23\n"
    "v_cmp_eq_u32 s[100:101], 1, v23\n"
    "s_nop 0\n"
    "v_cndmask_b32 v80, 0, v234, vcc\n"
    "v_cndmask_b32 v81, 0, v234, s[100:101]\n"
    "v_cmp_eq_u32 vcc, 2, v23\n"
    "v_cmp_eq_u32 s[100:101], 3, v23\n"
    "s_nop 0\n"
    "v_cndmask_b32 v82, 0, v234, vcc\n"
    "v_cndmask_b32 v83, 0, v234, s[100:101]\n"
    "v_cmp_eq_u32 vcc, 4, v23\n"
    "v_cmp_eq_u32 s[100:101], 5, v23\n"
    "s_nop 0\n"
    "v_cndmask_b32 v84, 0, v234, vcc\n"
    "v_cndmask_b32 v85, 0, v234, s[100:101]\n"
    "v_cmp_eq_u32 vcc, 6, v23\n"
    "v_cmp_eq_u32 s[100:101], 7, v23\n"
    "s_nop 0\n"
    "v_cndmask_b32 v86, 0, v234, vcc\n"
    "v_cndmask_b32 v87, 0, v234, s[100:101]\n"
    "s_cmp_lt_u32 s58, 28\n"
    "s_cbranch_scc0 Lsg_%=\n"
    "v_mov_b32 v10, v9\n"
    "s_waitcnt lgkmcnt(0)\n"
    "s_barrier\n"
    "ds_read_b64_tr_b16 v[176:177], v13 offset:0\n"
    "ds_read_b64_tr_b16 v[178:179], v14 offset:0\n"
    "ds_read_b64_tr_b16 v[180:181], v15 offset:0\n"
    "ds_read_b64_tr_b16 v[182:183], v30 offset:0\n"
    "ds_read_b128 v[160:163], v11 offset:0\n"
    "ds_read_b128 v[164:167], v11 offset:2048\n"
    "ds_read_b128 v[168:171], v11 offset:4096\n"
    "ds_read_b128 v[172:175], v11 offset:6144\n"
    "ds_read_b64_tr_b16 v[184:185], v13 offset:512\n"
    "ds_read_b64_tr_b16 v[186:187], v14 offset:512\n"
    "ds_read_b64_tr_b16 v[188:189], v15 offset:512\n"
    "ds_read_b64_tr_b16 v[190:191], v30 offset:512\n"
    "s_waitcnt lgkmcnt(10)\n"
    "s_waitcnt lgkmcnt(7)\n"
    "v_mfma_f32_16x16x32_bf16 v[96:99], v[176:179], v[160:163], 0\n"
    "ds_read_b64_tr_b16 v[208:209], v13 offset:8192\n"
    "v_mfma_f32_16x16x32_bf16 v[112:115], v[180:183], v[160:163], 0\n"
    "ds_read_b64_tr_b16 v[210:211], v14 offset:8192\n"
    "s_waitcnt lgkmcnt(8)\n"
    "v_mfma_f32_16x16x32_bf16 v[100:103], v[176:179], v[164:167], 0\n"
    "ds_read_b64_tr_b16 v[212:213], v15 offset:8192\n"
    "v_mfma_f32_16x16x32_bf16 v[116:119], v[180:183], v[164:167], 0\n"
    "ds_read_b64_tr_b16 v[214:215], v30 offset:8192\n"
    "s_waitcnt lgkmcnt(9)\n"
    "v_mfma_f32_16x16x32_bf16 v[104:107], v[176:179], v[168:171], 0\n"
    "ds_read_b128 v[192:195], v12 offset:0\n"
    "v_mfma_f32_16x16x32_bf16 v[120:123], v[180:183], v[168:171], 0\n"
    "ds_read_b128 v[196:199], v12 offset:2048\n"
    "s_waitcnt lgkmcnt(10)\n"
    "v_mfma_f32_16x16x32_bf16 v[108:111], v[176:179], v[172:175], 0\n"
    "ds_read_b128 v[200:203], v12 offset:4096\n"
    "v_mfma_f32_16x16x32_bf16 v[124:127], v[180:183], v[172:175], 0\n"
    "ds_read_b128 v[204:207], v12 offset:6144\n"
    "s_waitcnt lgkmcnt(10)\n"
    "v_mfma_f32_16x16x32_bf16 v[128:131], v[184:187], v[160:163], 0\n"
    "ds_read_b64_tr_b16 v[216:217], v13 offset:8704\n"
    "v_mfma_f32_16x16x32_bf16 v[132:135], v[184:187], v[164:167], 0\n"
    "ds_read_b64_tr_b16 v[218:219], v14 offset:8704\n"
    "v_mfma_f32_16x16x32_bf16 v[136:139], v[184:187], v[168:171], 0\n"
    "ds_read_b64_tr_b16 v[220:221], v15 offset:8704\n"
    "v_mfma_f32_16x16x32_bf16 v[140:143], v[184:187], v[172:175], 0\n"
    "ds_read_b64_tr_b16 v[222:223], v30 offset:8704\n"
    "s_waitcnt lgkmcnt(12)\n"
    "v_mfma_f32_16x16x32_bf16 v[144:147], v[188:191], v[160:163], 0\n"
    "v_mfma_f32_16x16x32_bf16 v[148:151], v[188:191], v[164:167], 0\n"
    "v_mfma_f32_16x16x32_bf16 v[152:155], v[188:191], v[168:171], 0\n"
    "v_mfma_f32_16x16x32_bf16 v[156:159], v[188:191], v[172:175], 0\n"
    "v_and_b32 v21, 3, v0\n"
    "v_mul_u32_u24 v21, 4160, v21\n"
    "v_lshrrev_b32 v22, 2, v4\n"
    "v_sub_u32 v22, v22, v5\n"
    "s_mul_i32 s77, s64, 16640\n"
    "s_add_u32 s77, s77, 98352\n"
    "v_lshl_add_u32 v21, v22, 4, v21\n"
    "v_add_u32 v240, s77, v21\n"
    "v_and_b32 v23, 1, v0\n"
    "v_xor_b32 v21, 1, v23\n"
    "v_mul_u32_u24 v21, 4640, v21\n"
    "v_lshrrev_b32 v22, 1, v4\n"
    "v_lshlrev_b32 v22, 6, v22\n"
    "v_lshl_add_u32 v22, v5, 4, v22\n"
    "s_mul_i32 s77, s64, 9280\n"
    "s_add_u32 s77, s77, 131584\n"
    "v_add3_u32 v241, v21, v22, s77\n"
    "v_lshl_add_u32 v242, v23, 3, v22\n"
    "v_add_u32 v243, s77, v242\n"
    "s_cmp_eq_u32 s65, 0\n"
    "s_movk_i32 s77, 0x1256\n"
    "s_cselect_b32 s77, 0x0347, s77\n"
    "v_lshlrev_b32 v21, 2, v5\n"
    "v_bfe_u32 v21, s77, v21, 3\n"
    "v_lshrrev_b32 v22, 2, v4\n"
    "v_lshl_add_u32 v21, v21, 2, v22\n"
    "v_and_b32 v22, 3, v0\n"
    "v_lshl_add_u32 v21, v21, 3, v22\n"
    "v_lshlrev_b32 v242, 4, v21\n"
    "s_mov_b32 s71, s67\n"
    "s_add_u32 s72, s67, 16384\n"
    "s_mul_i32 s77, s63, 14\n"
    "s_mul_i32 s78, s61, 56\n"
    "s_add_u32 s77, s77, s78\n"
    "s_lshl_b32 s70, s77, 12\n"
    "s_add_u32 s70, s70, 5242880\n"
    "s_lshl_b32 s77, s77, 7\n"
    "s_add_u32 s77, s77, 263168\n"
    "v_min_u32 v17, 2, v1\n"
    "v_lshl_add_u32 v17, v17, 8, s77\n"
    "v_lshrrev_b32 v21, 3, v0\n"
    "v_lshlrev_b32 v22, 4, v16\n"
    "s_waitcnt lgkmcnt(0)\n"
    "s_barrier\n"
    "v_mfma_f32_16x16x32_bf16 v[96:99], v[208:211], v[192:195], v[96:99]\n"
    "ds_read_b64_tr_b16 v[176:177], v13 offset:16384\n"
    "v_mfma_f32_16x16x32_bf16 v[112:115], v[212:215], v[192:195], v[112:115]\n"
    "ds_read_b64_tr_b16 v[178:179], v14 offset:16384\n"
    "v_mfma_f32_16x16x32_bf16 v[100:103], v[208:211], v[196:199], v[100:103]\n"
    "ds_read_b64_tr_b16 v[180:181], v15 offset:16384\n"
    "v_mfma_f32_16x16x32_bf16 v[116:119], v[212:215], v[196:199], v[116:119]\n"
    "ds_read_b64_tr_b16 v[182:183], v30 offset:16384\n"
    "v_mfma_f32_16x16x32_bf16 v[104:107], v[208:211], v[200:203], v[104:107]\n"
    "ds_read_b128 v[160:163], v11 offset:16384\n"
    "v_mfma_f32_16x16x32_bf16 v[120:123], v[212:215], v[200:203], v[120:123]\n"
    "ds_read_b128 v[164:167], v11 offset:18432\n"
    "v_mfma_f32_16x16x32_bf16 v[108:111], v[208:211], v[204:207], v[108:111]\n"
    "ds_read_b128 v[168:171], v11 offset:20480\n"
    "v_mfma_f32_16x16x32_bf16 v[124:127], v[212:215], v[204:207], v[124:127]\n"
    "ds_read_b128 v[172:175], v11 offset:22528\n"
    "v_mfma_f32_16x16x32_bf16 v[128:131], v[216:219], v[192:195], v[128:131]\n"
    "ds_read_b64_tr_b16 v[184:185], v13 offset:16896\n"
    "v_mfma_f32_16x16x32_bf16 v[132:135], v[216:219], v[196:199], v[132:135]\n"
    "ds_read_b64_tr_b16 v[186:187], v14 offset:16896\n"
    "v_mfma_f32_16x16x32_bf16 v[136:139], v[216:219], v[200:203], v[136:139]\n"
    "ds_read_b64_tr_b16 v[188:189], v15 offset:16896\n"
    "v_mfma_f32_16x16x32_bf16 v[140:143], v[216:219], v[204:207], v[140:143]\n"
    "ds_read_b64_tr_b16 v[190:191], v30 offset:16896\n"
    "v_mfma_f32_16x16x32_bf16 v[144:147], v[220:223], v[192:195], v[144:147]\n"
    "ds_read_b64_tr_b16 v[208:209], v13 offset:24576\n"
    "v_mfma_f32_16x16x32_bf16 v[148:151], v[220:223], v[196:199], v[148:151]\n"
    "ds_read_b64_tr_b16 v[210:211], v14 offset:24576\n"
    "v_mfma_f32_16x16x32_bf16 v[152:155], v[220:223], v[200:203], v[152:155]\n"
    "ds_read_b64_tr_b16 v[212:213], v15 offset:24576\n"
    "v_mfma_f32_16x16x32_bf16 v[156:159], v[220:223], v[204:207], v[156:159]\n"
    "ds_read_b64_tr_b16 v[214:215], v30 offset:24576\n"
    "s_waitcnt lgkmcnt(8)\n"
    "v_mfma_f32_16x16x32_bf16 v[96:99], v[176:179], v[160:163], v[96:99]\n"
    "ds_read_b128 v[192:195], v12 offset:16384\n"
    "v_mfma_f32_16x16x32_bf16 v[112:115], v[180:183], v[160:163], v[112:115]\n"
    "ds_read_b128 v[196:199], v12 offset:18432\n"
    "v_mfma_f32_16x16x32_bf16 v[100:103], v[176:179], v[164:167], v[100:103]\n"
    "ds_read_b128 v[200:203], v12 offset:20480\n"
    "v_mfma_f32_16x16x32_bf16 v[116:119], v[180:183], v[164:167], v[116:119]\n"
    "ds_read_b128 v[204:207], v12 offset:22528\n"
    "v_mfma_f32_16x16x32_bf16 v[104:107], v[176:179], v[168:171], v[104:107]\n"
    "ds_read_b64_tr_b16 v[216:217], v13 offset:25088\n"
    "v_mfma_f32_16x16x32_bf16 v[120:123], v[180:183], v[168:171], v[120:123]\n"
    "ds_read_b64_tr_b16 v[218:219], v14 offset:25088\n"
    "v_mfma_f32_16x16x32_bf16 v[108:111], v[176:179], v[172:175], v[108:111]\n"
    "ds_read_b64_tr_b16 v[220:221], v15 offset:25088\n"
    "v_mfma_f32_16x16x32_bf16 v[124:127], v[180:183], v[172:175], v[124:127]\n"
    "ds_read_b64_tr_b16 v[222:223], v30 offset:25088\n"
    "s_waitcnt lgkmcnt(12)\n"
    "v_mfma_f32_16x16x32_bf16 v[128:131], v[184:187], v[160:163], v[128:131]\n"
    "v_mfma_f32_16x16x32_bf16 v[132:135], v[184:187], v[164:167], v[132:135]\n"
    "v_mfma_f32_16x16x32_bf16 v[136:139], v[184:187], v[168:171], v[136:139]\n"
    "v_mfma_f32_16x16x32_bf16 v[140:143], v[184:187], v[172:175], v[140:143]\n"
    "v_mfma_f32_16x16x32_bf16 v[144:147], v[188:191], v[160:163], v[144:147]\n"
    "v_mfma_f32_16x16x32_bf16 v[148:151], v[188:191], v[164:167], v[148:151]\n"
    "v_mfma_f32_16x16x32_bf16 v[152:155], v[188:191], v[168:171], v[152:155]\n"
    "v_mfma_f32_16x16x32_bf16 v[156:159], v[188:191], v[172:175], v[156:159]\n"
    "s_lshl_b32 s77, s61, 16\n"
    "s_lshl_b32 s78, s62, 7\n"
    "s_add_u32 s77, s77, s78\n"
    "v_lshl_add_u32 v23, v21, 10, v22\n"
    "v_add_u32 v250, s77, v23\n"
    "v_add_u32 v254, s78, v22\n"
    "s_lshl_b32 s77, s62, 18\n"
    "s_lshl_b32 s78, s61, 7\n"
    "s_add_u32 s77, s77, s78\n"
    "s_add_u32 s77, s77, 3145728\n"
    "v_lshl_add_u32 v23, v21, 12, v22\n"
    "v_add_u32 v252, s77, v23\n"
    "s_lshl_b32 s77, s61, 10\n"
    "s_add_u32 s77, s77, 131072\n"
    "v_lshl_add_u32 v253, v21, 4, s77\n"
    "s_lshr_b32 s77, s61, 2\n"
    "s_lshr_b32 s77, 0x66, s77\n"
    "s_and_b32 s77, s77, 1\n"
    "s_lshl_b32 s77, s77, 16\n"
    "s_lshl3_add_u32 s78, s62, s63\n"
    "s_lshl_b32 s78, s78, 10\n"
    "s_add_u32 s77, s77, s78\n"
    "s_add_u32 s77, s77, 524288\n"
    "v_lshl_add_u32 v251, v16, 7, s77\n"
    "v_and_b32 v21, 8, v0\n"
    "v_lshl_add_u32 v251, v21, 9, v251\n"
    "v_xor_b32 v21, v16, v5\n"
    "v_lshlrev_b32 v21, 4, v21\n"
    "v_lshl_add_u32 v21, v4, 8, v21\n"
    "v_add_u32 v6, s71, v21\n"
    "v_add_u32 v24, s72, v21\n"
    "v_xor_b32 v20, 64, v6\n"
    "v_xor_b32 v25, 64, v24\n"
    "s_lshr_b32 s77, s57, 1\n"
    "s_mul_i32 s77, s77, 1\n"
    "s_add_u32 s28, s77, 0\n"
    "s_and_b32 s28, s28, 3\n"
    "s_lshl_b32 s28, s28, 13\n"
    "s_add_u32 s29, s77, 1\n"
    "s_and_b32 s29, s29, 3\n"
    "s_waitcnt lgkmcnt(0)\n"
    "s_barrier\n"
    "v_mfma_f32_16x16x32_bf16 v[96:99], v[208:211], v[192:195], v[96:99]\n"
    "ds_read_b64_tr_b16 v[176:177], v13 offset:32768\n"
    "v_mfma_f32_16x16x32_bf16 v[112:115], v[212:215], v[192:195], v[112:115]\n"
    "ds_read_b64_tr_b16 v[178:179], v14 offset:32768\n"
    "v_mfma_f32_16x16x32_bf16 v[100:103], v[208:211], v[196:199], v[100:103]\n"
    "ds_read_b64_tr_b16 v[180:181], v15 offset:32768\n"
    "v_mfma_f32_16x16x32_bf16 v[116:119], v[212:215], v[196:199], v[116:119]\n"
    "ds_read_b64_tr_b16 v[182:183], v30 offset:32768\n"
    "v_mfma_f32_16x16x32_bf16 v[104:107], v[208:211], v[200:203], v[104:107]\n"
    "ds_read_b128 v[160:163], v11 offset:32768\n"
    "v_mfma_f32_16x16x32_bf16 v[120:123], v[212:215], v[200:203], v[120:123]\n"
    "ds_read_b128 v[164:167], v11 offset:34816\n"
    "v_mfma_f32_16x16x32_bf16 v[108:111], v[208:211], v[204:207], v[108:111]\n"
    "ds_read_b128 v[168:171], v11 offset:36864\n"
    "v_mfma_f32_16x16x32_bf16 v[124:127], v[212:215], v[204:207], v[124:127]\n"
    "ds_read_b128 v[172:175], v11 offset:38912\n"
    "v_mfma_f32_16x16x32_bf16 v[128:131], v[216:219], v[192:195], v[128:131]\n"
    "ds_read_b64_tr_b16 v[184:185], v13 offset:33280\n"
    "v_mfma_f32_16x16x32_bf16 v[132:135], v[216:219], v[196:199], v[132:135]\n"
    "ds_read_b64_tr_b16 v[186:187], v14 offset:33280\n"
    "v_mfma_f32_16x16x32_bf16 v[136:139], v[216:219], v[200:203], v[136:139]\n"
    "ds_read_b64_tr_b16 v[188:189], v15 offset:33280\n"
    "v_mfma_f32_16x16x32_bf16 v[140:143], v[216:219], v[204:207], v[140:143]\n"
    "ds_read_b64_tr_b16 v[190:191], v30 offset:33280\n"
    "v_mfma_f32_16x16x32_bf16 v[144:147], v[220:223], v[192:195], v[144:147]\n"
    "ds_read_b64_tr_b16 v[208:209], v13 offset:40960\n"
    "v_mfma_f32_16x16x32_bf16 v[148:151], v[220:223], v[196:199], v[148:151]\n"
    "ds_read_b64_tr_b16 v[210:211], v14 offset:40960\n"
    "v_mfma_f32_16x16x32_bf16 v[152:155], v[220:223], v[200:203], v[152:155]\n"
    "ds_read_b64_tr_b16 v[212:213], v15 offset:40960\n"
    "v_mfma_f32_16x16x32_bf16 v[156:159], v[220:223], v[204:207], v[156:159]\n"
    "ds_read_b64_tr_b16 v[214:215], v30 offset:40960\n"
    "s_waitcnt lgkmcnt(8)\n"
    "v_mfma_f32_16x16x32_bf16 v[96:99], v[176:179], v[160:163], v[96:99]\n"
    "ds_read_b128 v[192:195], v12 offset:32768\n"
    "v_mfma_f32_16x16x32_bf16 v[112:115], v[180:183], v[160:163], v[112:115]\n"
    "ds_read_b128 v[196:199], v12 offset:34816\n"
    "v_mfma_f32_16x16x32_bf16 v[100:103], v[176:179], v[164:167], v[100:103]\n"
    "ds_read_b128 v[200:203], v12 offset:36864\n"
    "v_mfma_f32_16x16x32_bf16 v[116:119], v[180:183], v[164:167], v[116:119]\n"
    "ds_read_b128 v[204:207], v12 offset:38912\n"
    "v_mfma_f32_16x16x32_bf16 v[104:107], v[176:179], v[168:171], v[104:107]\n"
    "ds_read_b64_tr_b16 v[216:217], v13 offset:41472\n"
    "v_mfma_f32_16x16x32_bf16 v[120:123], v[180:183], v[168:171], v[120:123]\n"
    "ds_read_b64_tr_b16 v[218:219], v14 offset:41472\n"
    "v_mfma_f32_16x16x32_bf16 v[108:111], v[176:179], v[172:175], v[108:111]\n"
    "ds_read_b64_tr_b16 v[220:221], v15 offset:41472\n"
    "v_mfma_f32_16x16x32_bf16 v[124:127], v[180:183], v[172:175], v[124:127]\n"
    "ds_read_b64_tr_b16 v[222:223], v30 offset:41472\n"
    "s_waitcnt lgkmcnt(12)\n"
    "v_mfma_f32_16x16x32_bf16 v[128:131], v[184:187], v[160:163], v[128:131]\n"
    "v_mfma_f32_16x16x32_bf16 v[132:135], v[184:187], v[164:167], v[132:135]\n"
    "v_mfma_f32_16x16x32_bf16 v[136:139], v[184:187], v[168:171], v[136:139]\n"
    "v_mfma_f32_16x16x32_bf16 v[140:143], v[184:187], v[172:175], v[140:143]\n"
    "v_mfma_f32_16x16x32_bf16 v[144:147], v[188:191], v[160:163], v[144:147]\n"
    "v_mfma_f32_16x16x32_bf16 v[148:151], v[188:191], v[164:167], v[148:151]\n"
    "v_mfma_f32_16x16x32_bf16 v[152:155], v[188:191], v[168:171], v[152:155]\n"
    "v_mfma_f32_16x16x32_bf16 v[156:159], v[188:191], v[172:175], v[156:159]\n"
    "ds_read_b128 v[32:35], v224\n"
    "ds_read_b128 v[36:39], v225\n"
    "ds_read_b128 v[40:43], v226\n"
    "ds_read_b128 v[44:47], v227\n"
    "ds_read_b128 v[48:51], v228\n"
    "ds_read_b128 v[52:55], v229\n"
    "ds_read_b128 v[56:59], v230\n"
    "ds_read_b128 v[60:63], v231\n"
    "ds_read_b64 v[88:89], v232 offset:0\n"
    "ds_read_b64 v[90:91], v232 offset:128\n"
    "ds_read_b64 v[92:93], v232 offset:256\n"
    "ds_read_b64 v[94:95], v232 offset:384\n"
    "s_waitcnt lgkmcnt(11)\n"
    "v_mfma_f32_16x16x32_bf16 v[64:67], v[80:83], v[32:35], 0\n"
    "s_waitcnt lgkmcnt(10)\n"
    "v_mfma_f32_16x16x32_bf16 v[64:67], v[84:87], v[36:39], v[64:67]\n"
    "s_waitcnt lgkmcnt(9)\n"
    "v_mfma_f32_16x16x32_bf16 v[68:71], v[80:83], v[40:43], 0\n"
    "s_waitcnt lgkmcnt(8)\n"
    "v_mfma_f32_16x16x32_bf16 v[68:71], v[84:87], v[44:47], v[68:71]\n"
    "s_waitcnt lgkmcnt(7)\n"
    "v_mfma_f32_16x16x32_bf16 v[72:75], v[80:83], v[48:51], 0\n"
    "s_waitcnt lgkmcnt(6)\n"
    "v_mfma_f32_16x16x32_bf16 v[72:75], v[84:87], v[52:55], v[72:75]\n"
    "s_waitcnt lgkmcnt(5)\n"
    "v_mfma_f32_16x16x32_bf16 v[76:79], v[80:83], v[56:59], 0\n"
    "s_waitcnt lgkmcnt(4)\n"
    "v_mfma_f32_16x16x32_bf16 v[76:79], v[84:87], v[60:63], v[76:79]\n"
    "s_waitcnt lgkmcnt(0)\n"
    "v_lshlrev_b32 v32, 16, v88\n"
    "v_and_b32 v33, 0xffff0000, v88\n"
    "v_lshlrev_b32 v34, 16, v89\n"
    "v_and_b32 v35, 0xffff0000, v89\n"
    "v_lshlrev_b32 v36, 16, v90\n"
    "v_and_b32 v37, 0xffff0000, v90\n"
    "v_lshlrev_b32 v38, 16, v91\n"
    "v_and_b32 v39, 0xffff0000, v91\n"
    "v_lshlrev_b32 v40, 16, v92\n"
    "v_and_b32 v41, 0xffff0000, v92\n"
    "v_lshlrev_b32 v42, 16, v93\n"
    "v_and_b32 v43, 0xffff0000, v93\n"
    "v_lshlrev_b32 v44, 16, v94\n"
    "v_and_b32 v45, 0xffff0000, v94\n"
    "v_lshlrev_b32 v46, 16, v95\n"
    "v_and_b32 v47, 0xffff0000, v95\n"
    "v_pk_mul_f32 v[32:33], v[32:33], v[64:65]\n"
    "v_pk_mul_f32 v[34:35], v[34:35], v[66:67]\n"
    "v_pk_mul_f32 v[36:37], v[36:37], v[68:69]\n"
    "v_pk_mul_f32 v[38:39], v[38:39], v[70:71]\n"
    "v_pk_mul_f32 v[40:41], v[40:41], v[72:73]\n"
    "v_pk_mul_f32 v[42:43], v[42:43], v[74:75]\n"
    "v_pk_mul_f32 v[44:45], v[44:45], v[76:77]\n"
    "v_pk_mul_f32 v[46:47], v[46:47], v[78:79]\n"
    "v_cvt_pk_bf16_f32 v48, v32, v33\n"
    "v_cvt_pk_bf16_f32 v49, v36, v37\n"
    "v_cvt_pk_bf16_f32 v50, v40, v41\n"
    "v_cvt_pk_bf16_f32 v51, v44, v45\n"
    "buffer_store_dwordx4 v[48:51], v233, s[16:19], s36 offen offset:0 sc1\n"
    "v_cvt_pk_bf16_f32 v52, v34, v35\n"
    "v_cvt_pk_bf16_f32 v53, v38, v39\n"
    "v_cvt_pk_bf16_f32 v54, v42, v43\n"
    "v_cvt_pk_bf16_f32 v55, v46, v47\n"
    "buffer_store_dwordx4 v[52:55], v233, s[16:19], s36 offen offset:256 sc1\n"
    "s_waitcnt lgkmcnt(0)\n"
    "s_barrier\n"
    "v_mfma_f32_16x16x32_bf16 v[96:99], v[208:211], v[192:195], v[96:99]\n"
    "ds_read_b64_tr_b16 v[176:177], v13 offset:0\n"
    "v_mfma_f32_16x16x32_bf16 v[112:115], v[212:215], v[192:195], v[112:115]\n"
    "ds_read_b64_tr_b16 v[178:179], v14 offset:0\n"
    "v_mfma_f32_16x16x32_bf16 v[100:103], v[208:211], v[196:199], v[100:103]\n"
    "ds_read_b64_tr_b16 v[180:181], v15 offset:0\n"
    "v_mfma_f32_16x16x32_bf16 v[116:119], v[212:215], v[196:199], v[116:119]\n"
    "ds_read_b64_tr_b16 v[182:183], v30 offset:0\n"
    "v_mfma_f32_16x16x32_bf16 v[104:107], v[208:211], v[200:203], v[104:107]\n"
    "ds_read_b128 v[160:163], v11 offset:0\n"
    "v_mfma_f32_16x16x32_bf16 v[120:123], v[212:215], v[200:203], v[120:123]\n"
    "ds_read_b128 v[164:167], v11 offset:2048\n"
    "v_mfma_f32_16x16x32_bf16 v[108:111], v[208:211], v[204:207], v[108:111]\n"
    "ds_read_b128 v[168:171], v11 offset:4096\n"
    "v_mfma_f32_16x16x32_bf16 v[124:127], v[212:215], v[204:207], v[124:127]\n"
    "ds_read_b128 v[172:175], v11 offset:6144\n"
    "v_mfma_f32_16x16x32_bf16 v[128:131], v[216:219], v[192:195], v[128:131]\n"
    "ds_read_b64_tr_b16 v[184:185], v13 offset:512\n"
    "v_mfma_f32_16x16x32_bf16 v[132:135], v[216:219], v[196:199], v[132:135]\n"
    "ds_read_b64_tr_b16 v[186:187], v14 offset:512\n"
    "v_mfma_f32_16x16x32_bf16 v[136:139], v[216:219], v[200:203], v[136:139]\n"
    "ds_read_b64_tr_b16 v[188:189], v15 offset:512\n"
    "v_mfma_f32_16x16x32_bf16 v[140:143], v[216:219], v[204:207], v[140:143]\n"
    "ds_read_b64_tr_b16 v[190:191], v30 offset:512\n"
    "v_mfma_f32_16x16x32_bf16 v[144:147], v[220:223], v[192:195], v[144:147]\n"
    "ds_read_b64_tr_b16 v[208:209], v13 offset:8192\n"
    "v_mfma_f32_16x16x32_bf16 v[148:151], v[220:223], v[196:199], v[148:151]\n"
    "ds_read_b64_tr_b16 v[210:211], v14 offset:8192\n"
    "v_mfma_f32_16x16x32_bf16 v[152:155], v[220:223], v[200:203], v[152:155]\n"
    "ds_read_b64_tr_b16 v[212:213], v15 offset:8192\n"
    "v_mfma_f32_16x16x32_bf16 v[156:159], v[220:223], v[204:207], v[156:159]\n"
    "ds_read_b64_tr_b16 v[214:215], v30 offset:8192\n"
    "s_waitcnt lgkmcnt(8)\n"
    "v_mfma_f32_16x16x32_bf16 v[96:99], v[176:179], v[160:163], v[96:99]\n"
    "ds_read_b128 v[192:195], v12 offset:0\n"
    "v_mfma_f32_16x16x32_bf16 v[112:115], v[180:183], v[160:163], v[112:115]\n"
    "ds_read_b128 v[196:199], v12 offset:2048\n"
    "v_mfma_f32_16x16x32_bf16 v[100:103], v[176:179], v[164:167], v[100:103]\n"
    "ds_read_b128 v[200:203], v12 offset:4096\n"
    "v_mfma_f32_16x16x32_bf16 v[116:119], v[180:183], v[164:167], v[116:119]\n"
    "ds_read_b128 v[204:207], v12 offset:6144\n"
    "v_mfma_f32_16x16x32_bf16 v[104:107], v[176:179], v[168:171], v[104:107]\n"
    "ds_read_b64_tr_b16 v[216:217], v13 offset:8704\n"
    "v_mfma_f32_16x16x32_bf16 v[120:123], v[180:183], v[168:171], v[120:123]\n"
    "ds_read_b64_tr_b16 v[218:219], v14 offset:8704\n"
    "v_mfma_f32_16x16x32_bf16 v[108:111], v[176:179], v[172:175], v[108:111]\n"
    "ds_read_b64_tr_b16 v[220:221], v15 offset:8704\n"
    "v_mfma_f32_16x16x32_bf16 v[124:127], v[180:183], v[172:175], v[124:127]\n"
    "ds_read_b64_tr_b16 v[222:223], v30 offset:8704\n"
    "s_waitcnt lgkmcnt(12)\n"
    "v_mfma_f32_16x16x32_bf16 v[128:131], v[184:187], v[160:163], v[128:131]\n"
    "v_mfma_f32_16x16x32_bf16 v[132:135], v[184:187], v[164:167], v[132:135]\n"
    "v_mfma_f32_16x16x32_bf16 v[136:139], v[184:187], v[168:171], v[136:139]\n"
    "v_mfma_f32_16x16x32_bf16 v[140:143], v[184:187], v[172:175], v[140:143]\n"
    "v_mfma_f32_16x16x32_bf16 v[144:147], v[188:191], v[160:163], v[144:147]\n"
    "v_mfma_f32_16x16x32_bf16 v[148:151], v[188:191], v[164:167], v[148:151]\n"
    "v_mfma_f32_16x16x32_bf16 v[152:155], v[188:191], v[168:171], v[152:155]\n"
    "v_mfma_f32_16x16x32_bf16 v[156:159], v[188:191], v[172:175], v[156:159]\n"
    "v_mov_b32 v92, 0\n"
    "v_mov_b32 v93, 0\n"
    "v_mov_b32 v94, 0\n"
    "v_mov_b32 v95, 0\n"
    "s_mov_b64 exec, 0x1fffffff\n"
    "ds_write_b128 v239, v[92:95]\n"
    "s_mov_b64 exec, -1\n"
    "s_lshl_b32 s29, s29, 13\n"
    "s_add_u32 s30, s77, 2\n"
    "s_and_b32 s30, s30, 3\n"
    "s_lshl_b32 s30, s30, 13\n"
    "s_add_u32 s31, s77, 3\n"
    "s_and_b32 s31, s31, 3\n"
    "s_lshl_b32 s31, s31, 13\n"
    "s_lshl_b32 s77, s77, 1\n"
    "s_lshr_b32 s77, 0x924924, s77\n"
    "s_bfe_u32 s32, s77, 0x20000\n"
    "s_add_u32 s32, s32, 4\n"
    "s_lshl_b32 s32, s32, 13\n"
    "s_bfe_u32 s33, s77, 0x20002\n"
    "s_add_u32 s33, s33, 4\n"
    "s_lshl_b32 s33, s33, 13\n"
    "s_bfe_u32 s34, s77, 0x20004\n"
    "s_add_u32 s34, s34, 4\n"
    "s_lshl_b32 s34, s34, 13\n"
    "s_lshl_b32 s77, s64, 8\n"
    "s_add_u32 s77, s77, 152320\n"
    "v_lshl_add_u32 v21, v5, 4, s77\n"
    "s_lshl_b32 s77, s65, 10\n"
    "s_add_u32 s77, s77, 150272\n"
    "v_lshl_add_u32 v22, v4, 4, s77\n"
    "v_mov_b32 v244, 0xc0135761\n"
    "v_mov_b32 v245, 0xc0135761\n"
    "v_mov_b32 v246, 1.0\n"
    "v_mov_b32 v247, 1.0\n"
    "s_mov_b32 s98, 0xbdd2d3e8\n"
    "s_mov_b32 s99, s98\n"
    "s_waitcnt vmcnt(0)\n"
    "buffer_store_dwordx2 v[18:19], v9, s[16:19], s37 offen offset:0 sc1\n"
    "s_waitcnt lgkmcnt(0)\n"
    "s_barrier\n"
    "v_mfma_f32_16x16x32_bf16 v[96:99], v[208:211], v[192:195], v[96:99]\n"
    "ds_read_b64_tr_b16 v[176:177], v13 offset:16384\n"
    "v_mfma_f32_16x16x32_bf16 v[112:115], v[212:215], v[192:195], v[112:115]\n"
    "ds_read_b64_tr_b16 v[178:179], v14 offset:16384\n"
    "v_mfma_f32_16x16x32_bf16 v[100:103], v[208:211], v[196:199], v[100:103]\n"
    "ds_read_b64_tr_b16 v[180:181], v15 offset:16384\n"
    "v_mfma_f32_16x16x32_bf16 v[116:119], v[212:215], v[196:199], v[116:119]\n"
    "ds_read_b64_tr_b16 v[182:183], v30 offset:16384\n"
    "v_mfma_f32_16x16x32_bf16 v[104:107], v[208:211], v[200:203], v[104:107]\n"
    "ds_read_b128 v[160:163], v11 offset:16384\n"
    "v_mfma_f32_16x16x32_bf16 v[120:123], v[212:215], v[200:203], v[120:123]\n"
    "ds_read_b128 v[164:167], v11 offset:18432\n"
    "v_mfma_f32_16x16x32_bf16 v[108:111], v[208:211], v[204:207], v[108:111]\n"
    "ds_read_b128 v[168:171], v11 offset:20480\n"
    "v_mfma_f32_16x16x32_bf16 v[124:127], v[212:215], v[204:207], v[124:127]\n"
    "ds_read_b128 v[172:175], v11 offset:22528\n"
    "v_mfma_f32_16x16x32_bf16 v[128:131], v[216:219], v[192:195], v[128:131]\n"
    "ds_read_b64_tr_b16 v[184:185], v13 offset:16896\n"
    "v_mfma_f32_16x16x32_bf16 v[132:135], v[216:219], v[196:199], v[132:135]\n"
    "ds_read_b64_tr_b16 v[186:187], v14 offset:16896\n"
    "v_mfma_f32_16x16x32_bf16 v[136:139], v[216:219], v[200:203], v[136:139]\n"
    "ds_read_b64_tr_b16 v[188:189], v15 offset:16896\n"
    "v_mfma_f32_16x16x32_bf16 v[140:143], v[216:219], v[204:207], v[140:143]\n"
    "ds_read_b64_tr_b16 v[190:191], v30 offset:16896\n"
    "v_mfma_f32_16x16x32_bf16 v[144:147], v[220:223], v[192:195], v[144:147]\n"
    "ds_read_b64_tr_b16 v[208:209], v13 offset:24576\n"
    "v_mfma_f32_16x16x32_bf16 v[148:151], v[220:223], v[196:199], v[148:151]\n"
    "ds_read_b64_tr_b16 v[210:211], v14 offset:24576\n"
    "v_mfma_f32_16x16x32_bf16 v[152:155], v[220:223], v[200:203], v[152:155]\n"
    "ds_read_b64_tr_b16 v[212:213], v15 offset:24576\n"
    "v_mfma_f32_16x16x32_bf16 v[156:159], v[220:223], v[204:207], v[156:159]\n"
    "ds_read_b64_tr_b16 v[214:215], v30 offset:24576\n"
    "s_waitcnt lgkmcnt(8)\n"
    "v_mfma_f32_16x16x32_bf16 v[96:99], v[176:179], v[160:163], v[96:99]\n"
    "ds_read_b128 v[192:195], v12 offset:16384\n"
    "v_mfma_f32_16x16x32_bf16 v[112:115], v[180:183], v[160:163], v[112:115]\n"
    "ds_read_b128 v[196:199], v12 offset:18432\n"
    "v_mfma_f32_16x16x32_bf16 v[100:103], v[176:179], v[164:167], v[100:103]\n"
    "ds_read_b128 v[200:203], v12 offset:20480\n"
    "v_mfma_f32_16x16x32_bf16 v[116:119], v[180:183], v[164:167], v[116:119]\n"
    "ds_read_b128 v[204:207], v12 offset:22528\n"
    "v_mfma_f32_16x16x32_bf16 v[104:107], v[176:179], v[168:171], v[104:107]\n"
    "ds_read_b64_tr_b16 v[216:217], v13 offset:25088\n"
    "v_mfma_f32_16x16x32_bf16 v[120:123], v[180:183], v[168:171], v[120:123]\n"
    "ds_read_b64_tr_b16 v[218:219], v14 offset:25088\n"
    "v_mfma_f32_16x16x32_bf16 v[108:111], v[176:179], v[172:175], v[108:111]\n"
    "ds_read_b64_tr_b16 v[220:221], v15 offset:25088\n"
    "v_mfma_f32_16x16x32_bf16 v[124:127], v[180:183], v[172:175], v[124:127]\n"
    "ds_read_b64_tr_b16 v[222:223], v30 offset:25088\n"
    "s_waitcnt lgkmcnt(12)\n"
    "v_mfma_f32_16x16x32_bf16 v[128:131], v[184:187], v[160:163], v[128:131]\n"
    "v_mfma_f32_16x16x32_bf16 v[132:135], v[184:187], v[164:167], v[132:135]\n"
    "v_mfma_f32_16x16x32_bf16 v[136:139], v[184:187], v[168:171], v[136:139]\n"
    "v_mfma_f32_16x16x32_bf16 v[140:143], v[184:187], v[172:175], v[140:143]\n"
    "v_mfma_f32_16x16x32_bf16 v[144:147], v[188:191], v[160:163], v[144:147]\n"
    "v_mfma_f32_16x16x32_bf16 v[148:151], v[188:191], v[164:167], v[148:151]\n"
    "v_mfma_f32_16x16x32_bf16 v[152:155], v[188:191], v[168:171], v[152:155]\n"
    "v_mfma_f32_16x16x32_bf16 v[156:159], v[188:191], v[172:175], v[156:159]\n"
    "s_sleep 5\n"
    "s_waitcnt lgkmcnt(0)\n"
    "s_barrier\n"
    "v_mfma_f32_16x16x32_bf16 v[96:99], v[208:211], v[192:195], v[96:99]\n"
    "ds_read_b64_tr_b16 v[176:177], v13 offset:32768\n"
    "v_mfma_f32_16x16x32_bf16 v[112:115], v[212:215], v[192:195], v[112:115]\n"
    "ds_read_b64_tr_b16 v[178:179], v14 offset:32768\n"
    "v_mfma_f32_16x16x32_bf16 v[100:103], v[208:211], v[196:199], v[100:103]\n"
    "ds_read_b64_tr_b16 v[180:181], v15 offset:32768\n"
    "v_mfma_f32_16x16x32_bf16 v[116:119], v[212:215], v[196:199], v[116:119]\n"
    "ds_read_b64_tr_b16 v[182:183], v30 offset:32768\n"
    "v_mfma_f32_16x16x32_bf16 v[104:107], v[208:211], v[200:203], v[104:107]\n"
    "ds_read_b128 v[160:163], v11 offset:32768\n"
    "v_mfma_f32_16x16x32_bf16 v[120:123], v[212:215], v[200:203], v[120:123]\n"
    "ds_read_b128 v[164:167], v11 offset:34816\n"
    "v_mfma_f32_16x16x32_bf16 v[108:111], v[208:211], v[204:207], v[108:111]\n"
    "ds_read_b128 v[168:171], v11 offset:36864\n"
    "v_mfma_f32_16x16x32_bf16 v[124:127], v[212:215], v[204:207], v[124:127]\n"
    "ds_read_b128 v[172:175], v11 offset:38912\n"
    "v_mfma_f32_16x16x32_bf16 v[128:131], v[216:219], v[192:195], v[128:131]\n"
    "ds_read_b64_tr_b16 v[184:185], v13 offset:33280\n"
    "v_mfma_f32_16x16x32_bf16 v[132:135], v[216:219], v[196:199], v[132:135]\n"
    "ds_read_b64_tr_b16 v[186:187], v14 offset:33280\n"
    "v_mfma_f32_16x16x32_bf16 v[136:139], v[216:219], v[200:203], v[136:139]\n"
    "ds_read_b64_tr_b16 v[188:189], v15 offset:33280\n"
    "v_mfma_f32_16x16x32_bf16 v[140:143], v[216:219], v[204:207], v[140:143]\n"
    "ds_read_b64_tr_b16 v[190:191], v30 offset:33280\n"
    "v_mfma_f32_16x16x32_bf16 v[144:147], v[220:223], v[192:195], v[144:147]\n"
    "ds_read_b64_tr_b16 v[208:209], v13 offset:40960\n"
    "v_mfma_f32_16x16x32_bf16 v[148:151], v[220:223], v[196:199], v[148:151]\n"
    "ds_read_b64_tr_b16 v[210:211], v14 offset:40960\n"
    "v_mfma_f32_16x16x32_bf16 v[152:155], v[220:223], v[200:203], v[152:155]\n"
    "ds_read_b64_tr_b16 v[212:213], v15 offset:40960\n"
    "v_mfma_f32_16x16x32_bf16 v[156:159], v[220:223], v[204:207], v[156:159]\n"
    "ds_read_b64_tr_b16 v[214:215], v30 offset:40960\n"
    "s_waitcnt lgkmcnt(8)\n"
    "v_mfma_f32_16x16x32_bf16 v[96:99], v[176:179], v[160:163], v[96:99]\n"
    "ds_read_b128 v[192:195], v12 offset:32768\n"
    "v_mfma_f32_16x16x32_bf16 v[112:115], v[180:183], v[160:163], v[112:115]\n"
    "ds_read_b128 v[196:199], v12 offset:34816\n"
    "v_mfma_f32_16x16x32_bf16 v[100:103], v[176:179], v[164:167], v[100:103]\n"
    "ds_read_b128 v[200:203], v12 offset:36864\n"
    "v_mfma_f32_16x16x32_bf16 v[116:119], v[180:183], v[164:167], v[116:119]\n"
    "ds_read_b128 v[204:207], v12 offset:38912\n"
    "v_mfma_f32_16x16x32_bf16 v[104:107], v[176:179], v[168:171], v[104:107]\n"
    "ds_read_b64_tr_b16 v[216:217], v13 offset:41472\n"
    "v_mfma_f32_16x16x32_bf16 v[120:123], v[180:183], v[168:171], v[120:123]\n"
    "ds_read_b64_tr_b16 v[218:219], v14 offset:41472\n"
    "v_mfma_f32_16x16x32_bf16 v[108:111], v[176:179], v[172:175], v[108:111]\n"
    "ds_read_b64_tr_b16 v[220:221], v15 offset:41472\n"
    "v_mfma_f32_16x16x32_bf16 v[124:127], v[180:183], v[172:175], v[124:127]\n"
    "ds_read_b64_tr_b16 v[222:223], v30 offset:41472\n"
    "s_waitcnt lgkmcnt(12)\n"
    "v_mfma_f32_16x16x32_bf16 v[128:131], v[184:187], v[160:163], v[128:131]\n"
    "v_mfma_f32_16x16x32_bf16 v[132:135], v[184:187], v[164:167], v[132:135]\n"
    "v_mfma_f32_16x16x32_bf16 v[136:139], v[184:187], v[168:171], v[136:139]\n"
    "v_mfma_f32_16x16x32_bf16 v[140:143], v[184:187], v[172:175], v[140:143]\n"
    "v_mfma_f32_16x16x32_bf16 v[144:147], v[188:191], v[160:163], v[144:147]\n"
    "v_mfma_f32_16x16x32_bf16 v[148:151], v[188:191], v[164:167], v[148:151]\n"
    "v_mfma_f32_16x16x32_bf16 v[152:155], v[188:191], v[168:171], v[152:155]\n"
    "v_mfma_f32_16x16x32_bf16 v[156:159], v[188:191], v[172:175], v[156:159]\n"
    "s_sleep 5\n"
    "s_waitcnt lgkmcnt(0)\n"
    "s_barrier\n"
    "v_mfma_f32_16x16x32_bf16 v[96:99], v[208:211], v[192:195], v[96:99]\n"
    "ds_read_b64_tr_b16 v[176:177], v13 offset:0\n"
    "v_mfma_f32_16x16x32_bf16 v[112:115], v[212:215], v[192:195], v[112:115]\n"
    "ds_read_b64_tr_b16 v[178:179], v14 offset:0\n"
    "v_mfma_f32_16x16x32_bf16 v[100:103], v[208:211], v[196:199], v[100:103]\n"
    "ds_read_b64_tr_b16 v[180:181], v15 offset:0\n"
    "v_mfma_f32_16x16x32_bf16 v[116:119], v[212:215], v[196:199], v[116:119]\n"
    "ds_read_b64_tr_b16 v[182:183], v30 offset:0\n"
    "v_mfma_f32_16x16x32_bf16 v[104:107], v[208:211], v[200:203], v[104:107]\n"
    "ds_read_b128 v[160:163], v11 offset:0\n"
    "v_mfma_f32_16x16x32_bf16 v[120:123], v[212:215], v[200:203], v[120:123]\n"
    "ds_read_b128 v[164:167], v11 offset:2048\n"
    "v_mfma_f32_16x16x32_bf16 v[108:111], v[208:211], v[204:207], v[108:111]\n"
    "ds_read_b128 v[168:171], v11 offset:4096\n"
    "v_mfma_f32_16x16x32_bf16 v[124:127], v[212:215], v[204:207], v[124:127]\n"
    "ds_read_b128 v[172:175], v11 offset:6144\n"
    "v_mfma_f32_16x16x32_bf16 v[128:131], v[216:219], v[192:195], v[128:131]\n"
    "ds_read_b64_tr_b16 v[184:185], v13 offset:512\n"
    "v_mfma_f32_16x16x32_bf16 v[132:135], v[216:219], v[196:199], v[132:135]\n"
    "ds_read_b64_tr_b16 v[186:187], v14 offset:512\n"
    "v_mfma_f32_16x16x32_bf16 v[136:139], v[216:219], v[200:203], v[136:139]\n"
    "ds_read_b64_tr_b16 v[188:189], v15 offset:512\n"
    "v_mfma_f32_16x16x32_bf16 v[140:143], v[216:219], v[204:207], v[140:143]\n"
    "ds_read_b64_tr_b16 v[190:191], v30 offset:512\n"
    "v_mfma_f32_16x16x32_bf16 v[144:147], v[220:223], v[192:195], v[144:147]\n"
    "ds_read_b64_tr_b16 v[208:209], v13 offset:8192\n"
    "v_mfma_f32_16x16x32_bf16 v[148:151], v[220:223], v[196:199], v[148:151]\n"
    "ds_read_b64_tr_b16 v[210:211], v14 offset:8192\n"
    "v_mfma_f32_16x16x32_bf16 v[152:155], v[220:223], v[200:203], v[152:155]\n"
    "ds_read_b64_tr_b16 v[212:213], v15 offset:8192\n"
    "v_mfma_f32_16x16x32_bf16 v[156:159], v[220:223], v[204:207], v[156:159]\n"
    "ds_read_b64_tr_b16 v[214:215], v30 offset:8192\n"
    "s_waitcnt lgkmcnt(8)\n"
    "v_mfma_f32_16x16x32_bf16 v[96:99], v[176:179], v[160:163], v[96:99]\n"
    "ds_read_b128 v[192:195], v12 offset:0\n"
    "v_mfma_f32_16x16x32_bf16 v[112:115], v[180:183], v[160:163], v[112:115]\n"
    "ds_read_b128 v[196:199], v12 offset:2048\n"
    "v_mfma_f32_16x16x32_bf16 v[100:103], v[176:179], v[164:167], v[100:103]\n"
    "ds_read_b128 v[200:203], v12 offset:4096\n"
    "v_mfma_f32_16x16x32_bf16 v[116:119], v[180:183], v[164:167], v[116:119]\n"
    "ds_read_b128 v[204:207], v12 offset:6144\n"
    "v_mfma_f32_16x16x32_bf16 v[104:107], v[176:179], v[168:171], v[104:107]\n"
    "ds_read_b64_tr_b16 v[216:217], v13 offset:8704\n"
    "v_mfma_f32_16x16x32_bf16 v[120:123], v[180:183], v[168:171], v[120:123]\n"
    "ds_read_b64_tr_b16 v[218:219], v14 offset:8704\n"
    "v_mfma_f32_16x16x32_bf16 v[108:111], v[176:179], v[172:175], v[108:111]\n"
    "ds_read_b64_tr_b16 v[220:221], v15 offset:8704\n"
    "v_mfma_f32_16x16x32_bf16 v[124:127], v[180:183], v[172:175], v[124:127]\n"
    "ds_read_b64_tr_b16 v[222:223], v30 offset:8704\n"
    "s_waitcnt lgkmcnt(12)\n"
    "v_mfma_f32_16x16x32_bf16 v[128:131], v[184:187], v[160:163], v[128:131]\n"
    "v_mfma_f32_16x16x32_bf16 v[132:135], v[184:187], v[164:167], v[132:135]\n"
    "v_mfma_f32_16x16x32_bf16 v[136:139], v[184:187], v[168:171], v[136:139]\n"
    "v_mfma_f32_16x16x32_bf16 v[140:143], v[184:187], v[172:175], v[140:143]\n"
    "v_mfma_f32_16x16x32_bf16 v[144:147], v[188:191], v[160:163], v[144:147]\n"
    "v_mfma_f32_16x16x32_bf16 v[148:151], v[188:191], v[164:167], v[148:151]\n"
    "v_mfma_f32_16x16x32_bf16 v[152:155], v[188:191], v[168:171], v[152:155]\n"
    "v_mfma_f32_16x16x32_bf16 v[156:159], v[188:191], v[172:175], v[156:159]\n"
    "s_sleep 5\n"
    "s_waitcnt lgkmcnt(0)\n"
    "s_barrier\n"
    "v_mfma_f32_16x16x32_bf16 v[96:99], v[208:211], v[192:195], v[96:99]\n"
    "ds_read_b64_tr_b16 v[176:177], v13 offset:16384\n"
    "v_mfma_f32_16x16x32_bf16 v[112:115], v[212:215], v[192:195], v[112:115]\n"
    "ds_read_b64_tr_b16 v[178:179], v14 offset:16384\n"
    "v_mfma_f32_16x16x32_bf16 v[100:103], v[208:211], v[196:199], v[100:103]\n"
    "ds_read_b64_tr_b16 v[180:181], v15 offset:16384\n"
    "v_mfma_f32_16x16x32_bf16 v[116:119], v[212:215], v[196:199], v[116:119]\n"
    "ds_read_b64_tr_b16 v[182:183], v30 offset:16384\n"
    "v_mfma_f32_16x16x32_bf16 v[104:107], v[208:211], v[200:203], v[104:107]\n"
    "ds_read_b128 v[160:163], v11 offset:16384\n"
    "v_mfma_f32_16x16x32_bf16 v[120:123], v[212:215], v[200:203], v[120:123]\n"
    "ds_read_b128 v[164:167], v11 offset:18432\n"
    "v_mfma_f32_16x16x32_bf16 v[108:111], v[208:211], v[204:207], v[108:111]\n"
    "ds_read_b128 v[168:171], v11 offset:20480\n"
    "v_mfma_f32_16x16x32_bf16 v[124:127], v[212:215], v[204:207], v[124:127]\n"
    "ds_read_b128 v[172:175], v11 offset:22528\n"
    "v_mfma_f32_16x16x32_bf16 v[128:131], v[216:219], v[192:195], v[128:131]\n"
    "ds_read_b64_tr_b16 v[184:185], v13 offset:16896\n"
    "v_mfma_f32_16x16x32_bf16 v[132:135], v[216:219], v[196:199], v[132:135]\n"
    "ds_read_b64_tr_b16 v[186:187], v14 offset:16896\n"
    "v_mfma_f32_16x16x32_bf16 v[136:139], v[216:219], v[200:203], v[136:139]\n"
    "ds_read_b64_tr_b16 v[188:189], v15 offset:16896\n"
    "v_mfma_f32_16x16x32_bf16 v[140:143], v[216:219], v[204:207], v[140:143]\n"
    "ds_read_b64_tr_b16 v[190:191], v30 offset:16896\n"
    "v_mfma_f32_16x16x32_bf16 v[144:147], v[220:223], v[192:195], v[144:147]\n"
    "ds_read_b64_tr_b16 v[208:209], v13 offset:24576\n"
    "v_mfma_f32_16x16x32_bf16 v[148:151], v[220:223], v[196:199], v[148:151]\n"
    "ds_read_b64_tr_b16 v[210:211], v14 offset:24576\n"
    "v_mfma_f32_16x16x32_bf16 v[152:155], v[220:223], v[200:203], v[152:155]\n"
    "ds_read_b64_tr_b16 v[212:213], v15 offset:24576\n"
    "v_mfma_f32_16x16x32_bf16 v[156:159], v[220:223], v[204:207], v[156:159]\n"
    "ds_read_b64_tr_b16 v[214:215], v30 offset:24576\n"
    "s_waitcnt lgkmcnt(8)\n"
    "v_mfma_f32_16x16x32_bf16 v[96:99], v[176:179], v[160:163], v[96:99]\n"
    "ds_read_b128 v[192:195], v12 offset:16384\n"
    "v_mfma_f32_16x16x32_bf16 v[112:115], v[180:183], v[160:163], v[112:115]\n"
    "ds_read_b128 v[196:199], v12 offset:18432\n"
    "v_mfma_f32_16x16x32_bf16 v[100:103], v[176:179], v[164:167], v[100:103]\n"
    "ds_read_b128 v[200:203], v12 offset:20480\n"
    "v_mfma_f32_16x16x32_bf16 v[116:119], v[180:183], v[164:167], v[116:119]\n"
    "ds_read_b128 v[204:207], v12 offset:22528\n"
    "v_mfma_f32_16x16x32_bf16 v[104:107], v[176:179], v[168:171], v[104:107]\n"
    "ds_read_b64_tr_b16 v[216:217], v13 offset:25088\n"
    "v_mfma_f32_16x16x32_bf16 v[120:123], v[180:183], v[168:171], v[120:123]\n"
    "ds_read_b64_tr_b16 v[218:219], v14 offset:25088\n"
    "v_mfma_f32_16x16x32_bf16 v[108:111], v[176:179], v[172:175], v[108:111]\n"
    "ds_read_b64_tr_b16 v[220:221], v15 offset:25088\n"
    "v_mfma_f32_16x16x32_bf16 v[124:127], v[180:183], v[172:175], v[124:127]\n"
    "ds_read_b64_tr_b16 v[222:223], v30 offset:25088\n"
    "s_waitcnt lgkmcnt(12)\n"
    "v_mfma_f32_16x16x32_bf16 v[128:131], v[184:187], v[160:163], v[128:131]\n"
    "ds_write_b32 v27, v28 offset:0\n"
    "v_mfma_f32_16x16x32_bf16 v[132:135], v[184:187], v[164:167], v[132:135]\n"
    "v_mfma_f32_16x16x32_bf16 v[136:139], v[184:187], v[168:171], v[136:139]\n"
    "v_mfma_f32_16x16x32_bf16 v[140:143], v[184:187], v[172:175], v[140:143]\n"
    "v_mfma_f32_16x16x32_bf16 v[144:147], v[188:191], v[160:163], v[144:147]\n"
    "v_mfma_f32_16x16x32_bf16 v[148:151], v[188:191], v[164:167], v[148:151]\n"
    "v_mfma_f32_16x16x32_bf16 v[152:155], v[188:191], v[168:171], v[152:155]\n"
    "v_mfma_f32_16x16x32_bf16 v[156:159], v[188:191], v[172:175], v[156:159]\n"
    "s_waitcnt lgkmcnt(5)\n"
    "v_mfma_f32_16x16x32_bf16 v[96:99], v[208:211], v[192:195], v[96:99]\n"
    "ds_read_b128 v[32:35], v21 offset:0\n"
    "v_mfma_f32_16x16x32_bf16 v[112:115], v[212:215], v[192:195], v[112:115]\n"
    "ds_read_b128 v[36:39], v21 offset:64\n"
    "v_mfma_f32_16x16x32_bf16 v[100:103], v[208:211], v[196:199], v[100:103]\n"
    "ds_read_b128 v[40:43], v21 offset:128\n"
    "v_mfma_f32_16x16x32_bf16 v[116:119], v[212:215], v[196:199], v[116:119]\n"
    "ds_read_b128 v[44:47], v21 offset:192\n"
    "v_mfma_f32_16x16x32_bf16 v[104:107], v[208:211], v[200:203], v[104:107]\n"
    "ds_read_b128 v[48:51], v22 offset:0\n"
    "v_mfma_f32_16x16x32_bf16 v[120:123], v[212:215], v[200:203], v[120:123]\n"
    "ds_read_b128 v[52:55], v22 offset:256\n"
    "v_mfma_f32_16x16x32_bf16 v[108:111], v[208:211], v[204:207], v[108:111]\n"
    "ds_read_b128 v[56:59], v22 offset:512\n"
    "v_mfma_f32_16x16x32_bf16 v[124:127], v[212:215], v[204:207], v[124:127]\n"
    "ds_read_b128 v[60:63], v22 offset:768\n"
    "s_waitcnt lgkmcnt(9)\n"
    "v_mfma_f32_16x16x32_bf16 v[128:131], v[216:219], v[192:195], v[128:131]\n"
    "ds_read_b128 v[224:227], v21 offset:512\n"
    "v_mfma_f32_16x16x32_bf16 v[132:135], v[216:219], v[196:199], v[132:135]\n"
    "ds_read_b128 v[228:231], v21 offset:576\n"
    "v_mfma_f32_16x16x32_bf16 v[136:139], v[216:219], v[200:203], v[136:139]\n"
    "ds_read_b128 v[232:235], v21 offset:640\n"
    "v_mfma_f32_16x16x32_bf16 v[140:143], v[216:219], v[204:207], v[140:143]\n"
    "ds_read_b128 v[236:239], v21 offset:704\n"
    "v_mfma_f32_16x16x32_bf16 v[144:147], v[220:223], v[192:195], v[144:147]\n"
    "v_mfma_f32_16x16x32_bf16 v[148:151], v[220:223], v[196:199], v[148:151]\n"
    "v_mfma_f32_16x16x32_bf16 v[152:155], v[220:223], v[200:203], v[152:155]\n"
    "v_mfma_f32_16x16x32_bf16 v[156:159], v[220:223], v[204:207], v[156:159]\n"
    "s_waitcnt lgkmcnt(2)\n"
    "v_pk_fma_f32 v[72:73], v[224:225], v[50:51], v[32:33]\n"
    "v_pk_fma_f32 v[74:75], v[226:227], v[50:51], v[34:35]\n"
    "v_pk_fma_f32 v[76:77], v[228:229], v[50:51], v[36:37]\n"
    "v_pk_fma_f32 v[78:79], v[230:231], v[50:51], v[38:39]\n"
    "v_pk_fma_f32 v[64:65], v[96:97], v[48:49], v[72:73]\n"
    "v_pk_fma_f32 v[66:67], v[98:99], v[48:49], v[74:75]\n"
    "v_pk_fma_f32 v[68:69], v[112:113], v[48:49], v[76:77]\n"
    "v_pk_fma_f32 v[70:71], v[114:115], v[48:49], v[78:79]\n"
    "v_pk_mul_f32 v[72:73], v[64:65], v[64:65]\n"
    "v_pk_mul_f32 v[74:75], v[66:67], v[66:67]\n"
    "v_pk_mul_f32 v[76:77], v[68:69], v[68:69]\n"
    "v_pk_mul_f32 v[78:79], v[70:71], v[70:71]\n"
    "v_pk_fma_f32 v[72:73], v[72:73], s[98:99], v[244:245]\n"
    "v_pk_fma_f32 v[74:75], v[74:75], s[98:99], v[244:245]\n"
    "v_pk_fma_f32 v[76:77], v[76:77], s[98:99], v[244:245]\n"
    "v_pk_fma_f32 v[78:79], v[78:79], s[98:99], v[244:245]\n"
    "v_pk_mul_f32 v[72:73], v[64:65], v[72:73]\n"
    "v_pk_mul_f32 v[74:75], v[66:67], v[74:75]\n"
    "v_pk_mul_f32 v[76:77], v[68:69], v[76:77]\n"
    "v_pk_mul_f32 v[78:79], v[70:71], v[78:79]\n"
    "v_exp_f32 v72, v72\n"
    "v_exp_f32 v73, v73\n"
    "v_exp_f32 v74, v74\n"
    "v_exp_f32 v75, v75\n"
    "v_exp_f32 v76, v76\n"
    "v_exp_f32 v77, v77\n"
    "v_exp_f32 v78, v78\n"
    "v_exp_f32 v79, v79\n"
    "v_pk_add_f32 v[72:73], v[72:73], v[246:247]\n"
    "v_pk_add_f32 v[74:75], v[74:75], v[246:247]\n"
    "v_pk_add_f32 v[76:77], v[76:77], v[246:247]\n"
    "v_pk_add_f32 v[78:79], v[78:79], v[246:247]\n"
    "v_rcp_f32 v72, v72\n"
    "v_rcp_f32 v73, v73\n"
    "v_rcp_f32 v74, v74\n"
    "v_rcp_f32 v75, v75\n"
    "v_rcp_f32 v76, v76\n"
    "v_rcp_f32 v77, v77\n"
    "v_rcp_f32 v78, v78\n"
    "v_rcp_f32 v79, v79\n"
    "v_pk_mul_f32 v[64:65], v[64:65], v[72:73]\n"
    "v_pk_mul_f32 v[66:67], v[66:67], v[74:75]\n"
    "v_pk_mul_f32 v[68:69], v[68:69], v[76:77]\n"
    "v_pk_mul_f32 v[70:71], v[70:71], v[78:79]\n"
    "v_cvt_pk_bf16_f32 v80, v64, v65\n"
    "v_cvt_pk_bf16_f32 v81, v66, v67\n"
    "v_cvt_pk_bf16_f32 v82, v68, v69\n"
    "v_cvt_pk_bf16_f32 v83, v70, v71\n"
    "buffer_store_dwordx4 v[80:83], v2, s[16:19], s68 offen offset:0 sc1\n"
    "v_pk_fma_f32 v[72:73], v[224:225], v[54:55], v[32:33]\n"
    "v_pk_fma_f32 v[74:75], v[226:227], v[54:55], v[34:35]\n"
    "v_pk_fma_f32 v[76:77], v[228:229], v[54:55], v[36:37]\n"
    "v_pk_fma_f32 v[78:79], v[230:231], v[54:55], v[38:39]\n"
    "v_pk_fma_f32 v[64:65], v[100:101], v[52:53], v[72:73]\n"
    "v_pk_fma_f32 v[66:67], v[102:103], v[52:53], v[74:75]\n"
    "v_pk_fma_f32 v[68:69], v[116:117], v[52:53], v[76:77]\n"
    "v_pk_fma_f32 v[70:71], v[118:119], v[52:53], v[78:79]\n"
    "v_pk_mul_f32 v[72:73], v[64:65], v[64:65]\n"
    "v_pk_mul_f32 v[74:75], v[66:67], v[66:67]\n"
    "v_pk_mul_f32 v[76:77], v[68:69], v[68:69]\n"
    "v_pk_mul_f32 v[78:79], v[70:71], v[70:71]\n"
    "v_pk_fma_f32 v[72:73], v[72:73], s[98:99], v[244:245]\n"
    "v_pk_fma_f32 v[74:75], v[74:75], s[98:99], v[244:245]\n"
    "v_pk_fma_f32 v[76:77], v[76:77], s[98:99], v[244:245]\n"
    "v_pk_fma_f32 v[78:79], v[78:79], s[98:99], v[244:245]\n"
    "v_pk_mul_f32 v[72:73], v[64:65], v[72:73]\n"
    "v_pk_mul_f32 v[74:75], v[66:67], v[74:75]\n"
    "v_pk_mul_f32 v[76:77], v[68:69], v[76:77]\n"
    "v_pk_mul_f32 v[78:79], v[70:71], v[78:79]\n"
    "v_exp_f32 v72, v72\n"
    "v_exp_f32 v73, v73\n"
    "v_exp_f32 v74, v74\n"
    "v_exp_f32 v75, v75\n"
    "v_exp_f32 v76, v76\n"
    "v_exp_f32 v77, v77\n"
    "v_exp_f32 v78, v78\n"
    "v_exp_f32 v79, v79\n"
    "v_pk_add_f32 v[72:73], v[72:73], v[246:247]\n"
    "v_pk_add_f32 v[74:75], v[74:75], v[246:247]\n"
    "v_pk_add_f32 v[76:77], v[76:77], v[246:247]\n"
    "v_pk_add_f32 v[78:79], v[78:79], v[246:247]\n"
    "v_rcp_f32 v72, v72\n"
    "v_rcp_f32 v73, v73\n"
    "v_rcp_f32 v74, v74\n"
    "v_rcp_f32 v75, v75\n"
    "v_rcp_f32 v76, v76\n"
    "v_rcp_f32 v77, v77\n"
    "v_rcp_f32 v78, v78\n"
    "v_rcp_f32 v79, v79\n"
    "v_pk_mul_f32 v[64:65], v[64:65], v[72:73]\n"
    "v_pk_mul_f32 v[66:67], v[66:67], v[74:75]\n"
    "v_pk_mul_f32 v[68:69], v[68:69], v[76:77]\n"
    "v_pk_mul_f32 v[70:71], v[70:71], v[78:79]\n"
    "v_cvt_pk_bf16_f32 v84, v64, v65\n"
    "v_cvt_pk_bf16_f32 v85, v66, v67\n"
    "v_cvt_pk_bf16_f32 v86, v68, v69\n"
    "v_cvt_pk_bf16_f32 v87, v70, v71\n"
    "buffer_store_dwordx4 v[84:87], v2, s[16:19], s68 offen offset:1024 sc1\n"
    "v_pk_fma_f32 v[72:73], v[224:225], v[58:59], v[32:33]\n"
    "v_pk_fma_f32 v[74:75], v[226:227], v[58:59], v[34:35]\n"
    "v_pk_fma_f32 v[76:77], v[228:229], v[58:59], v[36:37]\n"
    "v_pk_fma_f32 v[78:79], v[230:231], v[58:59], v[38:39]\n"
    "v_pk_fma_f32 v[64:65], v[104:105], v[56:57], v[72:73]\n"
    "v_pk_fma_f32 v[66:67], v[106:107], v[56:57], v[74:75]\n"
    "v_pk_fma_f32 v[68:69], v[120:121], v[56:57], v[76:77]\n"
    "v_pk_fma_f32 v[70:71], v[122:123], v[56:57], v[78:79]\n"
    "v_pk_mul_f32 v[72:73], v[64:65], v[64:65]\n"
    "v_pk_mul_f32 v[74:75], v[66:67], v[66:67]\n"
    "v_pk_mul_f32 v[76:77], v[68:69], v[68:69]\n"
    "v_pk_mul_f32 v[78:79], v[70:71], v[70:71]\n"
    "v_pk_fma_f32 v[72:73], v[72:73], s[98:99], v[244:245]\n"
    "v_pk_fma_f32 v[74:75], v[74:75], s[98:99], v[244:245]\n"
    "v_pk_fma_f32 v[76:77], v[76:77], s[98:99], v[244:245]\n"
    "v_pk_fma_f32 v[78:79], v[78:79], s[98:99], v[244:245]\n"
    "v_pk_mul_f32 v[72:73], v[64:65], v[72:73]\n"
    "v_pk_mul_f32 v[74:75], v[66:67], v[74:75]\n"
    "v_pk_mul_f32 v[76:77], v[68:69], v[76:77]\n"
    "v_pk_mul_f32 v[78:79], v[70:71], v[78:79]\n"
    "v_exp_f32 v72, v72\n"
    "v_exp_f32 v73, v73\n"
    "v_exp_f32 v74, v74\n"
    "v_exp_f32 v75, v75\n"
    "v_exp_f32 v76, v76\n"
    "v_exp_f32 v77, v77\n"
    "v_exp_f32 v78, v78\n"
    "v_exp_f32 v79, v79\n"
    "v_pk_add_f32 v[72:73], v[72:73], v[246:247]\n"
    "v_pk_add_f32 v[74:75], v[74:75], v[246:247]\n"
    "v_pk_add_f32 v[76:77], v[76:77], v[246:247]\n"
    "v_pk_add_f32 v[78:79], v[78:79], v[246:247]\n"
    "v_rcp_f32 v72, v72\n"
    "v_rcp_f32 v73, v73\n"
    "v_rcp_f32 v74, v74\n"
    "v_rcp_f32 v75, v75\n"
    "v_rcp_f32 v76, v76\n"
    "v_rcp_f32 v77, v77\n"
    "v_rcp_f32 v78, v78\n"
    "v_rcp_f32 v79, v79\n"
    "v_pk_mul_f32 v[64:65], v[64:65], v[72:73]\n"
    "v_pk_mul_f32 v[66:67], v[66:67], v[74:75]\n"
    "v_pk_mul_f32 v[68:69], v[68:69], v[76:77]\n"
    "v_pk_mul_f32 v[70:71], v[70:71], v[78:79]\n"
    "v_cvt_pk_bf16_f32 v88, v64, v65\n"
    "v_cvt_pk_bf16_f32 v89, v66, v67\n"
    "v_cvt_pk_bf16_f32 v90, v68, v69\n"
    "v_cvt_pk_bf16_f32 v91, v70, v71\n"
    "buffer_store_dwordx4 v[88:91], v2, s[16:19], s68 offen offset:2048 sc1\n"
    "v_pk_fma_f32 v[72:73], v[224:225], v[62:63], v[32:33]\n"
    "v_pk_fma_f32 v[74:75], v[226:227], v[62:63], v[34:35]\n"
    "v_pk_fma_f32 v[76:77], v[228:229], v[62:63], v[36:37]\n"
    "v_pk_fma_f32 v[78:79], v[230:231], v[62:63], v[38:39]\n"
    "v_pk_fma_f32 v[64:65], v[108:109], v[60:61], v[72:73]\n"
    "v_pk_fma_f32 v[66:67], v[110:111], v[60:61], v[74:75]\n"
    "v_pk_fma_f32 v[68:69], v[124:125], v[60:61], v[76:77]\n"
    "v_pk_fma_f32 v[70:71], v[126:127], v[60:61], v[78:79]\n"
    "v_pk_mul_f32 v[72:73], v[64:65], v[64:65]\n"
    "v_pk_mul_f32 v[74:75], v[66:67], v[66:67]\n"
    "v_pk_mul_f32 v[76:77], v[68:69], v[68:69]\n"
    "v_pk_mul_f32 v[78:79], v[70:71], v[70:71]\n"
    "v_pk_fma_f32 v[72:73], v[72:73], s[98:99], v[244:245]\n"
    "v_pk_fma_f32 v[74:75], v[74:75], s[98:99], v[244:245]\n"
    "v_pk_fma_f32 v[76:77], v[76:77], s[98:99], v[244:245]\n"
    "v_pk_fma_f32 v[78:79], v[78:79], s[98:99], v[244:245]\n"
    "v_pk_mul_f32 v[72:73], v[64:65], v[72:73]\n"
    "v_pk_mul_f32 v[74:75], v[66:67], v[74:75]\n"
    "v_pk_mul_f32 v[76:77], v[68:69], v[76:77]\n"
    "v_pk_mul_f32 v[78:79], v[70:71], v[78:79]\n"
    "v_exp_f32 v72, v72\n"
    "v_exp_f32 v73, v73\n"
    "v_exp_f32 v74, v74\n"
    "v_exp_f32 v75, v75\n"
    "v_exp_f32 v76, v76\n"
    "v_exp_f32 v77, v77\n"
    "v_exp_f32 v78, v78\n"
    "v_exp_f32 v79, v79\n"
    "v_pk_add_f32 v[72:73], v[72:73], v[246:247]\n"
    "v_pk_add_f32 v[74:75], v[74:75], v[246:247]\n"
    "v_pk_add_f32 v[76:77], v[76:77], v[246:247]\n"
    "v_pk_add_f32 v[78:79], v[78:79], v[246:247]\n"
    "v_rcp_f32 v72, v72\n"
    "v_rcp_f32 v73, v73\n"
    "v_rcp_f32 v74, v74\n"
    "v_rcp_f32 v75, v75\n"
    "v_rcp_f32 v76, v76\n"
    "v_rcp_f32 v77, v77\n"
    "v_rcp_f32 v78, v78\n"
    "v_rcp_f32 v79, v79\n"
    "v_pk_mul_f32 v[64:65], v[64:65], v[72:73]\n"
    "v_pk_mul_f32 v[66:67], v[66:67], v[74:75]\n"
    "v_pk_mul_f32 v[68:69], v[68:69], v[76:77]\n"
    "v_pk_mul_f32 v[70:71], v[70:71], v[78:79]\n"
    "v_cvt_pk_bf16_f32 v80, v64, v65\n"
    "v_cvt_pk_bf16_f32 v81, v66, v67\n"
    "v_cvt_pk_bf16_f32 v82, v68, v69\n"
    "v_cvt_pk_bf16_f32 v83, v70, v71\n"
    "buffer_store_dwordx4 v[80:83], v2, s[16:19], s68 offen offset:3072 sc1\n"
    "s_waitcnt lgkmcnt(0)\n"
    "v_pk_fma_f32 v[72:73], v[232:233], v[50:51], v[40:41]\n"
    "v_pk_fma_f32 v[74:75], v[234:235], v[50:51], v[42:43]\n"
    "v_pk_fma_f32 v[76:77], v[236:237], v[50:51], v[44:45]\n"
    "v_pk_fma_f32 v[78:79], v[238:239], v[50:51], v[46:47]\n"
    "v_pk_fma_f32 v[64:65], v[128:129], v[48:49], v[72:73]\n"
    "v_pk_fma_f32 v[66:67], v[130:131], v[48:49], v[74:75]\n"
    "v_pk_fma_f32 v[68:69], v[144:145], v[48:49], v[76:77]\n"
    "v_pk_fma_f32 v[70:71], v[146:147], v[48:49], v[78:79]\n"
    "v_pk_mul_f32 v[72:73], v[64:65], v[64:65]\n"
    "v_pk_mul_f32 v[74:75], v[66:67], v[66:67]\n"
    "v_pk_mul_f32 v[76:77], v[68:69], v[68:69]\n"
    "v_pk_mul_f32 v[78:79], v[70:71], v[70:71]\n"
    "v_pk_fma_f32 v[72:73], v[72:73], s[98:99], v[244:245]\n"
    "v_pk_fma_f32 v[74:75], v[74:75], s[98:99], v[244:245]\n"
    "v_pk_fma_f32 v[76:77], v[76:77], s[98:99], v[244:245]\n"
    "v_pk_fma_f32 v[78:79], v[78:79], s[98:99], v[244:245]\n"
    "v_pk_mul_f32 v[72:73], v[64:65], v[72:73]\n"
    "v_pk_mul_f32 v[74:75], v[66:67], v[74:75]\n"
    "v_pk_mul_f32 v[76:77], v[68:69], v[76:77]\n"
    "v_pk_mul_f32 v[78:79], v[70:71], v[78:79]\n"
    "v_exp_f32 v72, v72\n"
    "v_exp_f32 v73, v73\n"
    "v_exp_f32 v74, v74\n"
    "v_exp_f32 v75, v75\n"
    "v_exp_f32 v76, v76\n"
    "v_exp_f32 v77, v77\n"
    "v_exp_f32 v78, v78\n"
    "v_exp_f32 v79, v79\n"
    "v_pk_add_f32 v[72:73], v[72:73], v[246:247]\n"
    "v_pk_add_f32 v[74:75], v[74:75], v[246:247]\n"
    "v_pk_add_f32 v[76:77], v[76:77], v[246:247]\n"
    "v_pk_add_f32 v[78:79], v[78:79], v[246:247]\n"
    "v_rcp_f32 v72, v72\n"
    "v_rcp_f32 v73, v73\n"
    "v_rcp_f32 v74, v74\n"
    "v_rcp_f32 v75, v75\n"
    "v_rcp_f32 v76, v76\n"
    "v_rcp_f32 v77, v77\n"
    "v_rcp_f32 v78, v78\n"
    "v_rcp_f32 v79, v79\n"
    "v_pk_mul_f32 v[64:65], v[64:65], v[72:73]\n"
    "v_pk_mul_f32 v[66:67], v[66:67], v[74:75]\n"
    "v_pk_mul_f32 v[68:69], v[68:69], v[76:77]\n"
    "v_pk_mul_f32 v[70:71], v[70:71], v[78:79]\n"
    "v_cvt_pk_bf16_f32 v84, v64, v65\n"
    "v_cvt_pk_bf16_f32 v85, v66, v67\n"
    "v_cvt_pk_bf16_f32 v86, v68, v69\n"
    "v_cvt_pk_bf16_f32 v87, v70, v71\n"
    "buffer_store_dwordx4 v[84:87], v2, s[16:19], s76 offen offset:0 sc1\n"
    "v_pk_fma_f32 v[72:73], v[232:233], v[54:55], v[40:41]\n"
    "v_pk_fma_f32 v[74:75], v[234:235], v[54:55], v[42:43]\n"
    "v_pk_fma_f32 v[76:77], v[236:237], v[54:55], v[44:45]\n"
    "v_pk_fma_f32 v[78:79], v[238:239], v[54:55], v[46:47]\n"
    "v_pk_fma_f32 v[64:65], v[132:133], v[52:53], v[72:73]\n"
    "v_pk_fma_f32 v[66:67], v[134:135], v[52:53], v[74:75]\n"
    "v_pk_fma_f32 v[68:69], v[148:149], v[52:53], v[76:77]\n"
    "v_pk_fma_f32 v[70:71], v[150:151], v[52:53], v[78:79]\n"
    "v_pk_mul_f32 v[72:73], v[64:65], v[64:65]\n"
    "v_pk_mul_f32 v[74:75], v[66:67], v[66:67]\n"
    "v_pk_mul_f32 v[76:77], v[68:69], v[68:69]\n"
    "v_pk_mul_f32 v[78:79], v[70:71], v[70:71]\n"
    "v_pk_fma_f32 v[72:73], v[72:73], s[98:99], v[244:245]\n"
    "v_pk_fma_f32 v[74:75], v[74:75], s[98:99], v[244:245]\n"
    "v_pk_fma_f32 v[76:77], v[76:77], s[98:99], v[244:245]\n"
    "v_pk_fma_f32 v[78:79], v[78:79], s[98:99], v[244:245]\n"
    "v_pk_mul_f32 v[72:73], v[64:65], v[72:73]\n"
    "v_pk_mul_f32 v[74:75], v[66:67], v[74:75]\n"
    "v_pk_mul_f32 v[76:77], v[68:69], v[76:77]\n"
    "v_pk_mul_f32 v[78:79], v[70:71], v[78:79]\n"
    "v_exp_f32 v72, v72\n"
    "v_exp_f32 v73, v73\n"
    "v_exp_f32 v74, v74\n"
    "v_exp_f32 v75, v75\n"
    "v_exp_f32 v76, v76\n"
    "v_exp_f32 v77, v77\n"
    "v_exp_f32 v78, v78\n"
    "v_exp_f32 v79, v79\n"
    "v_pk_add_f32 v[72:73], v[72:73], v[246:247]\n"
    "v_pk_add_f32 v[74:75], v[74:75], v[246:247]\n"
    "v_pk_add_f32 v[76:77], v[76:77], v[246:247]\n"
    "v_pk_add_f32 v[78:79], v[78:79], v[246:247]\n"
    "v_rcp_f32 v72, v72\n"
    "v_rcp_f32 v73, v73\n"
    "v_rcp_f32 v74, v74\n"
    "v_rcp_f32 v75, v75\n"
    "v_rcp_f32 v76, v76\n"
    "v_rcp_f32 v77, v77\n"
    "v_rcp_f32 v78, v78\n"
    "v_rcp_f32 v79, v79\n"
    "v_pk_mul_f32 v[64:65], v[64:65], v[72:73]\n"
    "v_pk_mul_f32 v[66:67], v[66:67], v[74:75]\n"
    "v_pk_mul_f32 v[68:69], v[68:69], v[76:77]\n"
    "v_pk_mul_f32 v[70:71], v[70:71], v[78:79]\n"
    "v_cvt_pk_bf16_f32 v88, v64, v65\n"
    "v_cvt_pk_bf16_f32 v89, v66, v67\n"
    "v_cvt_pk_bf16_f32 v90, v68, v69\n"
    "v_cvt_pk_bf16_f32 v91, v70, v71\n"
    "buffer_store_dwordx4 v[88:91], v2, s[16:19], s76 offen offset:1024 sc1\n"
    "v_pk_fma_f32 v[72:73], v[232:233], v[58:59], v[40:41]\n"
    "v_pk_fma_f32 v[74:75], v[234:235], v[58:59], v[42:43]\n"
    "v_pk_fma_f32 v[76:77], v[236:237], v[58:59], v[44:45]\n"
    "v_pk_fma_f32 v[78:79], v[238:239], v[58:59], v[46:47]\n"
    "v_pk_fma_f32 v[64:65], v[136:137], v[56:57], v[72:73]\n"
    "v_pk_fma_f32 v[66:67], v[138:139], v[56:57], v[74:75]\n"
    "v_pk_fma_f32 v[68:69], v[152:153], v[56:57], v[76:77]\n"
    "v_pk_fma_f32 v[70:71], v[154:155], v[56:57], v[78:79]\n"
    "v_pk_mul_f32 v[72:73], v[64:65], v[64:65]\n"
    "v_pk_mul_f32 v[74:75], v[66:67], v[66:67]\n"
    "v_pk_mul_f32 v[76:77], v[68:69], v[68:69]\n"
    "v_pk_mul_f32 v[78:79], v[70:71], v[70:71]\n"
    "v_pk_fma_f32 v[72:73], v[72:73], s[98:99], v[244:245]\n"
    "v_pk_fma_f32 v[74:75], v[74:75], s[98:99], v[244:245]\n"
    "v_pk_fma_f32 v[76:77], v[76:77], s[98:99], v[244:245]\n"
    "v_pk_fma_f32 v[78:79], v[78:79], s[98:99], v[244:245]\n"
    "v_pk_mul_f32 v[72:73], v[64:65], v[72:73]\n"
    "v_pk_mul_f32 v[74:75], v[66:67], v[74:75]\n"
    "v_pk_mul_f32 v[76:77], v[68:69], v[76:77]\n"
    "v_pk_mul_f32 v[78:79], v[70:71], v[78:79]\n"
    "v_exp_f32 v72, v72\n"
    "v_exp_f32 v73, v73\n"
    "v_exp_f32 v74, v74\n"
    "v_exp_f32 v75, v75\n"
    "v_exp_f32 v76, v76\n"
    "v_exp_f32 v77, v77\n"
    "v_exp_f32 v78, v78\n"
    "v_exp_f32 v79, v79\n"
    "v_pk_add_f32 v[72:73], v[72:73], v[246:247]\n"
    "v_pk_add_f32 v[74:75], v[74:75], v[246:247]\n"
    "v_pk_add_f32 v[76:77], v[76:77], v[246:247]\n"
    "v_pk_add_f32 v[78:79], v[78:79], v[246:247]\n"
    "v_rcp_f32 v72, v72\n"
    "v_rcp_f32 v73, v73\n"
    "v_rcp_f32 v74, v74\n"
    "v_rcp_f32 v75, v75\n"
    "v_rcp_f32 v76, v76\n"
    "v_rcp_f32 v77, v77\n"
    "v_rcp_f32 v78, v78\n"
    "v_rcp_f32 v79, v79\n"
    "v_pk_mul_f32 v[64:65], v[64:65], v[72:73]\n"
    "v_pk_mul_f32 v[66:67], v[66:67], v[74:75]\n"
    "v_pk_mul_f32 v[68:69], v[68:69], v[76:77]\n"
    "v_pk_mul_f32 v[70:71], v[70:71], v[78:79]\n"
    "v_cvt_pk_bf16_f32 v80, v64, v65\n"
    "v_cvt_pk_bf16_f32 v81, v66, v67\n"
    "v_cvt_pk_bf16_f32 v82, v68, v69\n"
    "v_cvt_pk_bf16_f32 v83, v70, v71\n"
    "buffer_store_dwordx4 v[80:83], v2, s[16:19], s76 offen offset:2048 sc1\n"
    "v_pk_fma_f32 v[72:73], v[232:233], v[62:63], v[40:41]\n"
    "v_pk_fma_f32 v[74:75], v[234:235], v[62:63], v[42:43]\n"
    "v_pk_fma_f32 v[76:77], v[236:237], v[62:63], v[44:45]\n"
    "v_pk_fma_f32 v[78:79], v[238:239], v[62:63], v[46:47]\n"
    "v_pk_fma_f32 v[64:65], v[140:141], v[60:61], v[72:73]\n"
    "v_pk_fma_f32 v[66:67], v[142:143], v[60:61], v[74:75]\n"
    "v_pk_fma_f32 v[68:69], v[156:157], v[60:61], v[76:77]\n"
    "v_pk_fma_f32 v[70:71], v[158:159], v[60:61], v[78:79]\n"
    "v_pk_mul_f32 v[72:73], v[64:65], v[64:65]\n"
    "v_pk_mul_f32 v[74:75], v[66:67], v[66:67]\n"
    "v_pk_mul_f32 v[76:77], v[68:69], v[68:69]\n"
    "v_pk_mul_f32 v[78:79], v[70:71], v[70:71]\n"
    "v_pk_fma_f32 v[72:73], v[72:73], s[98:99], v[244:245]\n"
    "v_pk_fma_f32 v[74:75], v[74:75], s[98:99], v[244:245]\n"
    "v_pk_fma_f32 v[76:77], v[76:77], s[98:99], v[244:245]\n"
    "v_pk_fma_f32 v[78:79], v[78:79], s[98:99], v[244:245]\n"
    "v_pk_mul_f32 v[72:73], v[64:65], v[72:73]\n"
    "v_pk_mul_f32 v[74:75], v[66:67], v[74:75]\n"
    "v_pk_mul_f32 v[76:77], v[68:69], v[76:77]\n"
    "v_pk_mul_f32 v[78:79], v[70:71], v[78:79]\n"
    "v_exp_f32 v72, v72\n"
    "v_exp_f32 v73, v73\n"
    "v_exp_f32 v74, v74\n"
    "v_exp_f32 v75, v75\n"
    "v_exp_f32 v76, v76\n"
    "v_exp_f32 v77, v77\n"
    "v_exp_f32 v78, v78\n"
    "v_exp_f32 v79, v79\n"
    "v_pk_add_f32 v[72:73], v[72:73], v[246:247]\n"
    "v_pk_add_f32 v[74:75], v[74:75], v[246:247]\n"
    "v_pk_add_f32 v[76:77], v[76:77], v[246:247]\n"
    "v_pk_add_f32 v[78:79], v[78:79], v[246:247]\n"
    "v_rcp_f32 v72, v72\n"
    "v_rcp_f32 v73, v73\n"
    "v_rcp_f32 v74, v74\n"
    "ds_read_b32 v26, v31 offset:16\n"
    "v_rcp_f32 v75, v75\n"
    "v_rcp_f32 v76, v76\n"
    "v_rcp_f32 v77, v77\n"
    "v_rcp_f32 v78, v78\n"
    "v_rcp_f32 v79, v79\n"
    "v_pk_mul_f32 v[64:65], v[64:65], v[72:73]\n"
    "v_pk_mul_f32 v[66:67], v[66:67], v[74:75]\n"
    "v_pk_mul_f32 v[68:69], v[68:69], v[76:77]\n"
    "v_pk_mul_f32 v[70:71], v[70:71], v[78:79]\n"
    "v_cvt_pk_bf16_f32 v84, v64, v65\n"
    "v_cvt_pk_bf16_f32 v85, v66, v67\n"
    "v_cvt_pk_bf16_f32 v86, v68, v69\n"
    "v_cvt_pk_bf16_f32 v87, v70, v71\n"
    "buffer_store_dwordx4 v[84:87], v2, s[16:19], s76 offen offset:3072 sc1\n"
    "s_waitcnt lgkmcnt(0)\n"
    "Lconv_%=:\n"
    "v_cmp_ne_u32 vcc, 1, v26\n"
    "s_cbranch_vccnz Lsp7_%=\n"
    "Lsb7_%=:\n"
    "v_cmp_ne_u32 vcc, 1, v26\n"
    "s_cbranch_vccnz Lsp8_%=\n"
    "Lsb8_%=:\n"
    "s_cmp_lg_u32 s65, 0\n"
    "s_cbranch_scc1 Lcv1_%=\n"
    "ds_read_b64 v[96:97], v243 offset:4032\n"
    "ds_read_b64 v[98:99], v243 offset:2496\n"
    "ds_read_b64 v[100:101], v243 offset:1984\n"
    "ds_read_b64 v[102:103], v243 offset:448\n"
    "ds_read_b128 v[188:191], v240 offset:3584\n"
    "ds_read_b128 v[192:195], v241 offset:448\n"
    "ds_read_b128 v[160:163], v240 offset:0\n"
    "ds_read_b128 v[220:223], v241 offset:4032\n"
    "ds_read_b128 v[176:179], v240 offset:2048\n"
    "ds_read_b128 v[208:211], v241 offset:2496\n"
    "ds_read_b128 v[172:175], v240 offset:1536\n"
    "ds_read_b128 v[204:207], v241 offset:1984\n"
    "ds_read_b128 v[196:199], v241 offset:960\n"
    "ds_read_b128 v[164:167], v240 offset:512\n"
    "ds_read_b128 v[184:187], v240 offset:3072\n"
    "ds_read_b128 v[216:219], v241 offset:3520\n"
    "ds_read_b128 v[168:171], v240 offset:1024\n"
    "ds_read_b128 v[200:203], v241 offset:1472\n"
    "ds_read_b128 v[180:183], v240 offset:2560\n"
    "ds_read_b128 v[212:215], v241 offset:3008\n"
    "s_lshl1_add_u32 s77, s55, s64\n"
    "s_lshl_b32 s74, s77, 12\n"
    "s_add_u32 s74, s74, 3145728\n"
    "s_lshl_b32 s77, s77, 7\n"
    "s_add_u32 s75, s77, 524288\n"
    "s_lshl_b32 s98, s54, 16\n"
    "s_and_b32 s79, s54, 0xffff0000\n"
    "s_cmp_eq_u32 s64, 0\n"
    "s_cselect_b32 s98, s98, s79\n"
    "s_waitcnt lgkmcnt(14)\n"
    "v_mfma_f32_16x16x32_bf16 v[224:227], v[188:191], v[192:195], 0\n"
    "ds_read_b128 v[188:191], v240 offset:3648\n"
    "s_waitcnt lgkmcnt(13)\n"
    "v_mfma_f32_16x16x32_bf16 v[224:227], v[160:163], v[220:223], v[224:227]\n"
    "ds_read_b128 v[220:223], v241 offset:3968\n"
    "v_lshlrev_b32 v104, 16, v96\n"
    "v_mfma_f32_16x16x32_bf16 v[236:239], v[160:163], v[192:195], 0\n"
    "v_and_b32 v105, 0xffff0000, v96\n"
    "s_waitcnt lgkmcnt(13)\n"
    "v_mfma_f32_16x16x32_bf16 v[228:231], v[176:179], v[192:195], 0\n"
    "v_lshlrev_b32 v106, 16, v97\n"
    "s_waitcnt lgkmcnt(12)\n"
    "v_mfma_f32_16x16x32_bf16 v[228:231], v[160:163], v[208:211], v[228:231]\n"
    "v_and_b32 v107, 0xffff0000, v97\n"
    "s_waitcnt lgkmcnt(11)\n"
    "v_mfma_f32_16x16x32_bf16 v[232:235], v[172:175], v[192:195], 0\n"
    "ds_read_b128 v[192:195], v241 offset:384\n"
    "v_lshlrev_b32 v108, 16, v98\n"
    "s_waitcnt lgkmcnt(11)\n"
    "v_mfma_f32_16x16x32_bf16 v[232:235], v[160:163], v[204:207], v[232:235]\n"
    "ds_read_b128 v[160:163], v240 offset:64\n"
    "v_and_b32 v109, 0xffff0000, v98\n"
    "v_mfma_f32_16x16x32_bf16 v[224:227], v[176:179], v[204:207], v[224:227]\n"
    "ds_read_b128 v[176:179], v240 offset:2112\n"
    "v_lshlrev_b32 v110, 16, v99\n"
    "v_mfma_f32_16x16x32_bf16 v[224:227], v[172:175], v[208:211], v[224:227]\n"
    "ds_read_b128 v[208:211], v241 offset:2432\n"
    "v_and_b32 v111, 0xffff0000, v99\n"
    "s_waitcnt lgkmcnt(13)\n"
    "v_mfma_f32_16x16x32_bf16 v[228:231], v[172:175], v[196:199], v[228:231]\n"
    "ds_read_b128 v[172:175], v240 offset:1600\n"
    "v_lshlrev_b32 v112, 16, v100\n"
    "s_waitcnt lgkmcnt(13)\n"
    "v_mfma_f32_16x16x32_bf16 v[228:231], v[164:167], v[204:207], v[228:231]\n"
    "ds_read_b128 v[204:207], v241 offset:1920\n"
    "v_and_b32 v113, 0xffff0000, v100\n"
    "s_waitcnt lgkmcnt(13)\n"
    "v_mfma_f32_16x16x32_bf16 v[224:227], v[184:187], v[196:199], v[224:227]\n"
    "ds_read_b128 v[184:187], v240 offset:3136\n"
    "v_lshlrev_b32 v114, 16, v101\n"
    "s_waitcnt lgkmcnt(13)\n"
    "v_mfma_f32_16x16x32_bf16 v[224:227], v[164:167], v[216:219], v[224:227]\n"
    "ds_read_b128 v[216:219], v241 offset:3456\n"
    "v_and_b32 v115, 0xffff0000, v101\n"
    "s_waitcnt lgkmcnt(13)\n"
    "v_mfma_f32_16x16x32_bf16 v[232:235], v[168:171], v[196:199], v[232:235]\n"
    "ds_read_b128 v[196:199], v241 offset:896\n"
    "v_lshlrev_b32 v116, 16, v102\n"
    "s_waitcnt lgkmcnt(13)\n"
    "v_mfma_f32_16x16x32_bf16 v[232:235], v[164:167], v[200:203], v[232:235]\n"
    "ds_read_b128 v[164:167], v240 offset:576\n"
    "v_and_b32 v117, 0xffff0000, v102\n"
    "v_mfma_f32_16x16x32_bf16 v[228:231], v[168:171], v[200:203], v[228:231]\n"
    "v_lshlrev_b32 v118, 16, v103\n"
    "s_waitcnt lgkmcnt(13)\n"
    "v_mfma_f32_16x16x32_bf16 v[224:227], v[180:183], v[200:203], v[224:227]\n"
    "ds_read_b128 v[180:183], v240 offset:2624\n"
    "ds_read_b128 v[200:203], v241 offset:1408\n"
    "v_and_b32 v119, 0xffff0000, v103\n"
    "s_waitcnt lgkmcnt(14)\n"
    "v_mfma_f32_16x16x32_bf16 v[224:227], v[168:171], v[212:215], v[224:227]\n"
    "ds_read_b128 v[168:171], v240 offset:1088\n"
    "ds_read_b128 v[212:215], v241 offset:2944\n"
    "s_waitcnt lgkmcnt(13)\n"
    "v_mfma_f32_16x16x32_bf16 v[224:227], v[188:191], v[192:195], v[224:227]\n"
    "ds_read_b128 v[188:191], v240 offset:3712\n"
    "s_waitcnt lgkmcnt(13)\n"
    "v_mfma_f32_16x16x32_bf16 v[224:227], v[160:163], v[220:223], v[224:227]\n"
    "ds_read_b128 v[220:223], v241 offset:3904\n"
    "v_mfma_f32_16x16x32_bf16 v[236:239], v[160:163], v[192:195], v[236:239]\n"
    "ds_write_b32 v27, v28 offset:64\n"
    "s_waitcnt vmcnt(0)\n"
    "buffer_store_dwordx2 v[18:19], v10, s[16:19], s69 offen offset:128 sc1\n"
    "s_waitcnt lgkmcnt(14)\n"
    "v_mfma_f32_16x16x32_bf16 v[228:231], v[176:179], v[192:195], v[228:231]\n"
    "s_waitcnt lgkmcnt(13)\n"
    "v_mfma_f32_16x16x32_bf16 v[228:231], v[160:163], v[208:211], v[228:231]\n"
    "s_waitcnt lgkmcnt(12)\n"
    "v_mfma_f32_16x16x32_bf16 v[232:235], v[172:175], v[192:195], v[232:235]\n"
    "ds_read_b128 v[192:195], v241 offset:320\n"
    "s_waitcnt lgkmcnt(12)\n"
    "v_mfma_f32_16x16x32_bf16 v[232:235], v[160:163], v[204:207], v[232:235]\n"
    "ds_read_b128 v[160:163], v240 offset:128\n"
    "v_mfma_f32_16x16x32_bf16 v[224:227], v[176:179], v[204:207], v[224:227]\n"
    "ds_read_b128 v[176:179], v240 offset:2176\n"
    "v_mfma_f32_16x16x32_bf16 v[224:227], v[172:175], v[208:211], v[224:227]\n"
    "ds_read_b128 v[208:211], v241 offset:2368\n"
    "s_waitcnt lgkmcnt(12)\n"
    "v_mfma_f32_16x16x32_bf16 v[228:231], v[172:175], v[196:199], v[228:231]\n"
    "ds_read_b128 v[172:175], v240 offset:1664\n"
    "s_waitcnt lgkmcnt(12)\n"
    "v_mfma_f32_16x16x32_bf16 v[228:231], v[164:167], v[204:207], v[228:231]\n"
    "ds_read_b128 v[204:207], v241 offset:1856\n"
    "v_mfma_f32_16x16x32_bf16 v[224:227], v[184:187], v[196:199], v[224:227]\n"
    "ds_read_b128 v[184:187], v240 offset:3200\n"
    "v_mfma_f32_16x16x32_bf16 v[224:227], v[164:167], v[216:219], v[224:227]\n"
    "ds_read_b128 v[216:219], v241 offset:3392\n"
    "s_waitcnt lgkmcnt(12)\n"
    "v_mfma_f32_16x16x32_bf16 v[232:235], v[168:171], v[196:199], v[232:235]\n"
    "ds_read_b128 v[196:199], v241 offset:832\n"
    "v_mfma_f32_16x16x32_bf16 v[232:235], v[164:167], v[200:203], v[232:235]\n"
    "ds_read_b128 v[164:167], v240 offset:640\n"
    "v_mfma_f32_16x16x32_bf16 v[228:231], v[168:171], v[200:203], v[228:231]\n"
    "v_mfma_f32_16x16x32_bf16 v[224:227], v[180:183], v[200:203], v[224:227]\n"
    "ds_read_b128 v[180:183], v240 offset:2688\n"
    "ds_read_b128 v[200:203], v241 offset:1344\n"
    "s_waitcnt lgkmcnt(15)\n"
    "v_mfma_f32_16x16x32_bf16 v[224:227], v[168:171], v[212:215], v[224:227]\n"
    "ds_read_b128 v[168:171], v240 offset:1152\n"
    "ds_read_b128 v[212:215], v241 offset:2880\n"
    "s_waitcnt lgkmcnt(13)\n"
    "v_mfma_f32_16x16x32_bf16 v[224:227], v[188:191], v[192:195], v[224:227]\n"
    "ds_read_b128 v[188:191], v240 offset:3776\n"
    "s_waitcnt lgkmcnt(13)\n"
    "v_mfma_f32_16x16x32_bf16 v[224:227], v[160:163], v[220:223], v[224:227]\n"
    "ds_read_b128 v[220:223], v241 offset:3840\n"
    "v_mfma_f32_16x16x32_bf16 v[236:239], v[160:163], v[192:195], v[236:239]\n"
    "s_waitcnt lgkmcnt(13)\n"
    "v_mfma_f32_16x16x32_bf16 v[228:231], v[176:179], v[192:195], v[228:231]\n"
    "s_waitcnt lgkmcnt(12)\n"
    "v_mfma_f32_16x16x32_bf16 v[228:231], v[160:163], v[208:211], v[228:231]\n"
    "s_waitcnt lgkmcnt(11)\n"
    "v_mfma_f32_16x16x32_bf16 v[232:235], v[172:175], v[192:195], v[232:235]\n"
    "ds_read_b128 v[192:195], v241 offset:256\n"
    "s_waitcnt lgkmcnt(11)\n"
    "v_mfma_f32_16x16x32_bf16 v[232:235], v[160:163], v[204:207], v[232:235]\n"
    "ds_read_b128 v[160:163], v240 offset:192\n"
    "v_mfma_f32_16x16x32_bf16 v[224:227], v[176:179], v[204:207], v[224:227]\n"
    "ds_read_b128 v[176:179], v240 offset:2240\n"
    "v_mfma_f32_16x16x32_bf16 v[224:227], v[172:175], v[208:211], v[224:227]\n"
    "ds_read_b128 v[208:211], v241 offset:2304\n"
    "s_waitcnt lgkmcnt(11)\n"
    "v_mfma_f32_16x16x32_bf16 v[228:231], v[172:175], v[196:199], v[228:231]\n"
    "ds_read_b128 v[172:175], v240 offset:1728\n"
    "s_waitcnt lgkmcnt(11)\n"
    "v_mfma_f32_16x16x32_bf16 v[228:231], v[164:167], v[204:207], v[228:231]\n"
    "ds_read_b128 v[204:207], v241 offset:1792\n"
    "v_mfma_f32_16x16x32_bf16 v[224:227], v[184:187], v[196:199], v[224:227]\n"
    "ds_read_b128 v[184:187], v240 offset:3264\n"
    "v_mfma_f32_16x16x32_bf16 v[224:227], v[164:167], v[216:219], v[224:227]\n"
    "ds_read_b128 v[216:219], v241 offset:3328\n"
    "s_waitcnt lgkmcnt(11)\n"
    "v_mfma_f32_16x16x32_bf16 v[232:235], v[168:171], v[196:199], v[232:235]\n"
    "ds_read_b128 v[196:199], v241 offset:768\n"
    "v_mfma_f32_16x16x32_bf16 v[232:235], v[164:167], v[200:203], v[232:235]\n"
    "ds_read_b128 v[164:167], v240 offset:704\n"
    "v_mfma_f32_16x16x32_bf16 v[228:231], v[168:171], v[200:203], v[228:231]\n"
    "v_mfma_f32_16x16x32_bf16 v[224:227], v[180:183], v[200:203], v[224:227]\n"
    "ds_read_b128 v[180:183], v240 offset:2752\n"
    "ds_read_b128 v[200:203], v241 offset:1280\n"
    "s_waitcnt lgkmcnt(14)\n"
    "v_mfma_f32_16x16x32_bf16 v[224:227], v[168:171], v[212:215], v[224:227]\n"
    "ds_read_b128 v[168:171], v240 offset:1216\n"
    "ds_read_b128 v[212:215], v241 offset:2816\n"
    "s_waitcnt lgkmcnt(13)\n"
    "v_mfma_f32_16x16x32_bf16 v[224:227], v[188:191], v[192:195], v[224:227]\n"
    "ds_read_b128 v[188:191], v240 offset:3840\n"
    "s_waitcnt lgkmcnt(13)\n"
    "v_mfma_f32_16x16x32_bf16 v[224:227], v[160:163], v[220:223], v[224:227]\n"
    "ds_read_b128 v[220:223], v241 offset:3776\n"
    "v_mfma_f32_16x16x32_bf16 v[236:239], v[160:163], v[192:195], v[236:239]\n"
    "s_waitcnt lgkmcnt(13)\n"
    "v_mfma_f32_16x16x32_bf16 v[228:231], v[176:179], v[192:195], v[228:231]\n"
    "s_waitcnt lgkmcnt(12)\n"
    "v_mfma_f32_16x16x32_bf16 v[228:231], v[160:163], v[208:211], v[228:231]\n"
    "s_waitcnt lgkmcnt(11)\n"
    "v_mfma_f32_16x16x32_bf16 v[232:235], v[172:175], v[192:195], v[232:235]\n"
    "ds_read_b128 v[192:195], v241 offset:192\n"
    "s_waitcnt lgkmcnt(11)\n"
    "v_mfma_f32_16x16x32_bf16 v[232:235], v[160:163], v[204:207], v[232:235]\n"
    "ds_read_b128 v[160:163], v240 offset:256\n"
    "v_mfma_f32_16x16x32_bf16 v[224:227], v[176:179], v[204:207], v[224:227]\n"
    "ds_read_b128 v[176:179], v240 offset:2304\n"
    "v_mfma_f32_16x16x32_bf16 v[224:227], v[172:175], v[208:211], v[224:227]\n"
    "ds_read_b128 v[208:211], v241 offset:2240\n"
    "s_waitcnt lgkmcnt(11)\n"
    "v_mfma_f32_16x16x32_bf16 v[228:231], v[172:175], v[196:199], v[228:231]\n"
    "ds_read_b128 v[172:175], v240 offset:1792\n"
    "s_waitcnt lgkmcnt(11)\n"
    "v_mfma_f32_16x16x32_bf16 v[228:231], v[164:167], v[204:207], v[228:231]\n"
    "ds_read_b128 v[204:207], v241 offset:1728\n"
    "v_mfma_f32_16x16x32_bf16 v[224:227], v[184:187], v[196:199], v[224:227]\n"
    "ds_read_b128 v[184:187], v240 offset:3328\n"
    "v_mfma_f32_16x16x32_bf16 v[224:227], v[164:167], v[216:219], v[224:227]\n"
    "ds_read_b128 v[216:219], v241 offset:3264\n"
    "s_waitcnt lgkmcnt(11)\n"
    "v_mfma_f32_16x16x32_bf16 v[232:235], v[168:171], v[196:199], v[232:235]\n"
    "ds_read_b128 v[196:199], v241 offset:704\n"
    "v_mfma_f32_16x16x32_bf16 v[232:235], v[164:167], v[200:203], v[232:235]\n"
    "ds_read_b128 v[164:167], v240 offset:768\n"
    "v_mfma_f32_16x16x32_bf16 v[228:231], v[168:171], v[200:203], v[228:231]\n"
    "v_mfma_f32_16x16x32_bf16 v[224:227], v[180:183], v[200:203], v[224:227]\n"
    "ds_read_b128 v[180:183], v240 offset:2816\n"
    "ds_read_b128 v[200:203], v241 offset:1216\n"
    "s_waitcnt lgkmcnt(14)\n"
    "v_mfma_f32_16x16x32_bf16 v[224:227], v[168:171], v[212:215], v[224:227]\n"
    "ds_read_b128 v[168:171], v240 offset:1280\n"
    "ds_read_b128 v[212:215], v241 offset:2752\n"
    "s_waitcnt lgkmcnt(13)\n"
    "v_mfma_f32_16x16x32_bf16 v[224:227], v[188:191], v[192:195], v[224:227]\n"
    "ds_read_b128 v[188:191], v240 offset:3904\n"
    "s_waitcnt lgkmcnt(13)\n"
    "v_mfma_f32_16x16x32_bf16 v[224:227], v[160:163], v[220:223], v[224:227]\n"
    "ds_read_b128 v[220:223], v241 offset:3712\n"
    "v_mfma_f32_16x16x32_bf16 v[236:239], v[160:163], v[192:195], v[236:239]\n"
    "s_waitcnt lgkmcnt(13)\n"
    "v_mfma_f32_16x16x32_bf16 v[228:231], v[176:179], v[192:195], v[228:231]\n"
    "s_waitcnt lgkmcnt(12)\n"
    "v_mfma_f32_16x16x32_bf16 v[228:231], v[160:163], v[208:211], v[228:231]\n"
    "s_waitcnt lgkmcnt(11)\n"
    "v_mfma_f32_16x16x32_bf16 v[232:235], v[172:175], v[192:195], v[232:235]\n"
    "ds_read_b128 v[192:195], v241 offset:128\n"
    "s_waitcnt lgkmcnt(11)\n"
    "v_mfma_f32_16x16x32_bf16 v[232:235], v[160:163], v[204:207], v[232:235]\n"
    "ds_read_b128 v[160:163], v240 offset:320\n"
    "v_mfma_f32_16x16x32_bf16 v[224:227], v[176:179], v[204:207], v[224:227]\n"
    "ds_read_b128 v[176:179], v240 offset:2368\n"
    "v_mfma_f32_16x16x32_bf16 v[224:227], v[172:175], v[208:211], v[224:227]\n"
    "ds_read_b128 v[208:211], v241 offset:2176\n"
    "s_waitcnt lgkmcnt(11)\n"
    "v_mfma_f32_16x16x32_bf16 v[228:231], v[172:175], v[196:199], v[228:231]\n"
    "ds_read_b128 v[172:175], v240 offset:1856\n"
    "s_waitcnt lgkmcnt(11)\n"
    "v_mfma_f32_16x16x32_bf16 v[228:231], v[164:167], v[204:207], v[228:231]\n"
    "ds_read_b128 v[204:207], v241 offset:1664\n"
    "v_mfma_f32_16x16x32_bf16 v[224:227], v[184:187], v[196:199], v[224:227]\n"
    "ds_read_b128 v[184:187], v240 offset:3392\n"
    "v_mfma_f32_16x16x32_bf16 v[224:227], v[164:167], v[216:219], v[224:227]\n"
    "ds_read_b128 v[216:219], v241 offset:3200\n"
    "s_waitcnt lgkmcnt(11)\n"
    "v_mfma_f32_16x16x32_bf16 v[232:235], v[168:171], v[196:199], v[232:235]\n"
    "ds_read_b128 v[196:199], v241 offset:640\n"
    "v_mfma_f32_16x16x32_bf16 v[232:235], v[164:167], v[200:203], v[232:235]\n"
    "ds_read_b128 v[164:167], v240 offset:832\n"
    "v_mfma_f32_16x16x32_bf16 v[228:231], v[168:171], v[200:203], v[228:231]\n"
    "v_mfma_f32_16x16x32_bf16 v[224:227], v[180:183], v[200:203], v[224:227]\n"
    "ds_read_b128 v[180:183], v240 offset:2880\n"
    "ds_read_b128 v[200:203], v241 offset:1152\n"
    "s_waitcnt lgkmcnt(14)\n"
    "v_mfma_f32_16x16x32_bf16 v[224:227], v[168:171], v[212:215], v[224:227]\n"
    "ds_read_b128 v[168:171], v240 offset:1344\n"
    "ds_read_b128 v[212:215], v241 offset:2688\n"
    "s_waitcnt lgkmcnt(13)\n"
    "v_mfma_f32_16x16x32_bf16 v[224:227], v[188:191], v[192:195], v[224:227]\n"
    "ds_read_b128 v[188:191], v240 offset:3968\n"
    "s_waitcnt lgkmcnt(13)\n"
    "v_mfma_f32_16x16x32_bf16 v[224:227], v[160:163], v[220:223], v[224:227]\n"
    "ds_read_b128 v[220:223], v241 offset:3648\n"
    "v_mfma_f32_16x16x32_bf16 v[236:239], v[160:163], v[192:195], v[236:239]\n"
    "s_waitcnt lgkmcnt(13)\n"
    "v_mfma_f32_16x16x32_bf16 v[228:231], v[176:179], v[192:195], v[228:231]\n"
    "s_waitcnt lgkmcnt(12)\n"
    "v_mfma_f32_16x16x32_bf16 v[228:231], v[160:163], v[208:211], v[228:231]\n"
    "s_waitcnt lgkmcnt(11)\n"
    "v_mfma_f32_16x16x32_bf16 v[232:235], v[172:175], v[192:195], v[232:235]\n"
    "ds_read_b128 v[192:195], v241 offset:64\n"
    "s_waitcnt lgkmcnt(11)\n"
    "v_mfma_f32_16x16x32_bf16 v[232:235], v[160:163], v[204:207], v[232:235]\n"
    "ds_read_b128 v[160:163], v240 offset:384\n"
    "v_mfma_f32_16x16x32_bf16 v[224:227], v[176:179], v[204:207], v[224:227]\n"
    "ds_read_b128 v[176:179], v240 offset:2432\n"
    "v_mfma_f32_16x16x32_bf16 v[224:227], v[172:175], v[208:211], v[224:227]\n"
    "ds_read_b128 v[208:211], v241 offset:2112\n"
    "s_waitcnt lgkmcnt(11)\n"
    "v_mfma_f32_16x16x32_bf16 v[228:231], v[172:175], v[196:199], v[228:231]\n"
    "ds_read_b128 v[172:175], v240 offset:1920\n"
    "s_waitcnt lgkmcnt(11)\n"
    "v_mfma_f32_16x16x32_bf16 v[228:231], v[164:167], v[204:207], v[228:231]\n"
    "ds_read_b128 v[204:207], v241 offset:1600\n"
    "v_mfma_f32_16x16x32_bf16 v[224:227], v[184:187], v[196:199], v[224:227]\n"
    "ds_read_b128 v[184:187], v240 offset:3456\n"
    "v_mfma_f32_16x16x32_bf16 v[224:227], v[164:167], v[216:219], v[224:227]\n"
    "ds_read_b128 v[216:219], v241 offset:3136\n"
    "s_waitcnt lgkmcnt(11)\n"
    "v_mfma_f32_16x16x32_bf16 v[232:235], v[168:171], v[196:199], v[232:235]\n"
    "ds_read_b128 v[196:199], v241 offset:576\n"
    "v_mfma_f32_16x16x32_bf16 v[232:235], v[164:167], v[200:203], v[232:235]\n"
    "ds_read_b128 v[164:167], v240 offset:896\n"
    "v_mfma_f32_16x16x32_bf16 v[228:231], v[168:171], v[200:203], v[228:231]\n"
    "v_mfma_f32_16x16x32_bf16 v[224:227], v[180:183], v[200:203], v[224:227]\n"
    "ds_read_b128 v[180:183], v240 offset:2944\n"
    "ds_read_b128 v[200:203], v241 offset:1088\n"
    "s_waitcnt lgkmcnt(14)\n"
    "v_mfma_f32_16x16x32_bf16 v[224:227], v[168:171], v[212:215], v[224:227]\n"
    "ds_read_b128 v[168:171], v240 offset:1408\n"
    "ds_read_b128 v[212:215], v241 offset:2624\n"
    "s_waitcnt lgkmcnt(13)\n"
    "v_mfma_f32_16x16x32_bf16 v[224:227], v[188:191], v[192:195], v[224:227]\n"
    "ds_read_b128 v[188:191], v240 offset:4032\n"
    "s_waitcnt lgkmcnt(13)\n"
    "v_mfma_f32_16x16x32_bf16 v[224:227], v[160:163], v[220:223], v[224:227]\n"
    "ds_read_b128 v[220:223], v241 offset:3584\n"
    "v_mfma_f32_16x16x32_bf16 v[236:239], v[160:163], v[192:195], v[236:239]\n"
    "s_waitcnt lgkmcnt(13)\n"
    "v_mfma_f32_16x16x32_bf16 v[228:231], v[176:179], v[192:195], v[228:231]\n"
    "s_waitcnt lgkmcnt(12)\n"
    "v_mfma_f32_16x16x32_bf16 v[228:231], v[160:163], v[208:211], v[228:231]\n"
    "s_waitcnt lgkmcnt(11)\n"
    "v_mfma_f32_16x16x32_bf16 v[232:235], v[172:175], v[192:195], v[232:235]\n"
    "ds_read_b128 v[192:195], v241 offset:0\n"
    "s_waitcnt lgkmcnt(11)\n"
    "v_mfma_f32_16x16x32_bf16 v[232:235], v[160:163], v[204:207], v[232:235]\n"
    "ds_read_b128 v[160:163], v240 offset:448\n"
    "v_mfma_f32_16x16x32_bf16 v[224:227], v[176:179], v[204:207], v[224:227]\n"
    "ds_read_b128 v[176:179], v240 offset:2496\n"
    "v_mfma_f32_16x16x32_bf16 v[224:227], v[172:175], v[208:211], v[224:227]\n"
    "ds_read_b128 v[208:211], v241 offset:2048\n"
    "s_waitcnt lgkmcnt(11)\n"
    "v_mfma_f32_16x16x32_bf16 v[228:231], v[172:175], v[196:199], v[228:231]\n"
    "ds_read_b128 v[172:175], v240 offset:1984\n"
    "s_waitcnt lgkmcnt(11)\n"
    "v_mfma_f32_16x16x32_bf16 v[228:231], v[164:167], v[204:207], v[228:231]\n"
    "ds_read_b128 v[204:207], v241 offset:1536\n"
    "v_mfma_f32_16x16x32_bf16 v[224:227], v[184:187], v[196:199], v[224:227]\n"
    "ds_read_b128 v[184:187], v240 offset:3520\n"
    "v_mfma_f32_16x16x32_bf16 v[224:227], v[164:167], v[216:219], v[224:227]\n"
    "ds_read_b128 v[216:219], v241 offset:3072\n"
    "s_waitcnt lgkmcnt(11)\n"
    "v_mfma_f32_16x16x32_bf16 v[232:235], v[168:171], v[196:199], v[232:235]\n"
    "ds_read_b128 v[196:199], v241 offset:512\n"
    "v_mfma_f32_16x16x32_bf16 v[232:235], v[164:167], v[200:203], v[232:235]\n"
    "ds_read_b128 v[164:167], v240 offset:960\n"
    "v_mfma_f32_16x16x32_bf16 v[228:231], v[168:171], v[200:203], v[228:231]\n"
    "v_mfma_f32_16x16x32_bf16 v[224:227], v[180:183], v[200:203], v[224:227]\n"
    "ds_read_b128 v[180:183], v240 offset:3008\n"
    "ds_read_b128 v[200:203], v241 offset:1024\n"
    "s_waitcnt lgkmcnt(14)\n"
    "v_mfma_f32_16x16x32_bf16 v[224:227], v[168:171], v[212:215], v[224:227]\n"
    "ds_read_b128 v[168:171], v240 offset:1472\n"
    "ds_read_b128 v[212:215], v241 offset:2560\n"
    "s_waitcnt lgkmcnt(13)\n"
    "v_mfma_f32_16x16x32_bf16 v[224:227], v[188:191], v[192:195], v[224:227]\n"
    "s_waitcnt lgkmcnt(12)\n"
    "v_mfma_f32_16x16x32_bf16 v[224:227], v[160:163], v[220:223], v[224:227]\n"
    "v_mfma_f32_16x16x32_bf16 v[236:239], v[160:163], v[192:195], v[236:239]\n"
    "s_waitcnt lgkmcnt(11)\n"
    "v_mfma_f32_16x16x32_bf16 v[228:231], v[176:179], v[192:195], v[228:231]\n"
    "s_waitcnt lgkmcnt(10)\n"
    "v_mfma_f32_16x16x32_bf16 v[228:231], v[160:163], v[208:211], v[228:231]\n"
    "s_waitcnt lgkmcnt(9)\n"
    "v_mfma_f32_16x16x32_bf16 v[232:235], v[172:175], v[192:195], v[232:235]\n"
    "s_waitcnt lgkmcnt(8)\n"
    "v_mfma_f32_16x16x32_bf16 v[232:235], v[160:163], v[204:207], v[232:235]\n"
    "v_mfma_f32_16x16x32_bf16 v[224:227], v[176:179], v[204:207], v[224:227]\n"
    "v_mfma_f32_16x16x32_bf16 v[224:227], v[172:175], v[208:211], v[224:227]\n"
    "s_waitcnt lgkmcnt(5)\n"
    "v_mfma_f32_16x16x32_bf16 v[228:231], v[172:175], v[196:199], v[228:231]\n"
    "s_waitcnt lgkmcnt(4)\n"
    "v_mfma_f32_16x16x32_bf16 v[228:231], v[164:167], v[204:207], v[228:231]\n"
    "v_mfma_f32_16x16x32_bf16 v[224:227], v[184:187], v[196:199], v[224:227]\n"
    "v_mfma_f32_16x16x32_bf16 v[224:227], v[164:167], v[216:219], v[224:227]\n"
    "s_waitcnt lgkmcnt(1)\n"
    "v_mfma_f32_16x16x32_bf16 v[232:235], v[168:171], v[196:199], v[232:235]\n"
    "v_mfma_f32_16x16x32_bf16 v[232:235], v[164:167], v[200:203], v[232:235]\n"
    "v_mfma_f32_16x16x32_bf16 v[228:231], v[168:171], v[200:203], v[228:231]\n"
    "v_mfma_f32_16x16x32_bf16 v[224:227], v[180:183], v[200:203], v[224:227]\n"
    "s_waitcnt lgkmcnt(0)\n"
    "v_mfma_f32_16x16x32_bf16 v[224:227], v[168:171], v[212:215], v[224:227]\n"
    "s_setprio 0\n"
    "v_fma_f32 v116, v116, s98, v236\n"
    "v_fma_f32 v117, v117, s98, v237\n"
    "v_fma_f32 v118, v118, s98, v238\n"
    "v_fma_f32 v119, v119, s98, v239\n"
    "v_cvt_pk_bf16_f32 v102, v116, v117\n"
    "v_cvt_pk_bf16_f32 v103, v118, v119\n"
    "v_fma_f32 v112, v112, s98, v232\n"
    "v_fma_f32 v113, v113, s98, v233\n"
    "v_fma_f32 v114, v114, s98, v234\n"
    "v_fma_f32 v115, v115, s98, v235\n"
    "v_cvt_pk_bf16_f32 v100, v112, v113\n"
    "v_cvt_pk_bf16_f32 v101, v114, v115\n"
    "v_fma_f32 v108, v108, s98, v228\n"
    "v_fma_f32 v109, v109, s98, v229\n"
    "v_fma_f32 v110, v110, s98, v230\n"
    "v_fma_f32 v111, v111, s98, v231\n"
    "v_cvt_pk_bf16_f32 v98, v108, v109\n"
    "v_cvt_pk_bf16_f32 v99, v110, v111\n"
    "v_fma_f32 v104, v104, s98, v224\n"
    "v_fma_f32 v105, v105, s98, v225\n"
    "v_fma_f32 v106, v106, s98, v226\n"
    "v_fma_f32 v107, v107, s98, v227\n"
    "v_cvt_pk_bf16_f32 v96, v104, v105\n"
    "v_cvt_pk_bf16_f32 v97, v106, v107\n"
    "s_nop 0\n"
    "v_permlane32_swap_b32 v96, v100\n"
    "v_permlane32_swap_b32 v97, v101\n"
    "v_permlane32_swap_b32 v98, v102\n"
    "v_permlane32_swap_b32 v99, v103\n"
    "s_nop 0\n"
    "v_permlane16_swap_b32 v96, v98\n"
    "v_permlane16_swap_b32 v97, v99\n"
    "v_permlane16_swap_b32 v100, v102\n"
    "v_permlane16_swap_b32 v101, v103\n"
    "buffer_store_dwordx4 v[96:99], v242, s[16:19], s74 offen offset:0 sc1\n"
    "buffer_store_dwordx4 v[100:103], v242, s[16:19], s74 offen offset:64 sc1\n"
    "s_waitcnt lgkmcnt(0)\n"
    "s_branch Lcvd_%=\n"
    "Lcv1_%=:\n"
    "ds_read_b64 v[96:97], v243 offset:3520\n"
    "ds_read_b64 v[98:99], v243 offset:3008\n"
    "ds_read_b64 v[100:101], v243 offset:1472\n"
    "ds_read_b64 v[102:103], v243 offset:960\n"
    "ds_read_b128 v[184:187], v240 offset:3072\n"
    "ds_read_b128 v[192:195], v241 offset:448\n"
    "ds_read_b128 v[160:163], v240 offset:0\n"
    "ds_read_b128 v[216:219], v241 offset:3520\n"
    "ds_read_b128 v[180:183], v240 offset:2560\n"
    "ds_read_b128 v[212:215], v241 offset:3008\n"
    "ds_read_b128 v[168:171], v240 offset:1024\n"
    "ds_read_b128 v[200:203], v241 offset:1472\n"
    "ds_read_b128 v[164:167], v240 offset:512\n"
    "ds_read_b128 v[196:199], v241 offset:960\n"
    "ds_read_b128 v[176:179], v240 offset:2048\n"
    "ds_read_b128 v[208:211], v241 offset:2496\n"
    "ds_read_b128 v[172:175], v240 offset:1536\n"
    "ds_read_b128 v[204:207], v241 offset:1984\n"
    "s_lshl1_add_u32 s77, s55, s64\n"
    "s_lshl_b32 s74, s77, 12\n"
    "s_add_u32 s74, s74, 3145728\n"
    "s_lshl_b32 s77, s77, 7\n"
    "s_add_u32 s75, s77, 589824\n"
    "s_lshl_b32 s98, s54, 16\n"
    "s_and_b32 s79, s54, 0xffff0000\n"
    "s_cmp_eq_u32 s64, 0\n"
    "s_cselect_b32 s98, s98, s79\n"
    "s_waitcnt lgkmcnt(12)\n"
    "v_mfma_f32_16x16x32_bf16 v[224:227], v[184:187], v[192:195], 0\n"
    "ds_read_b128 v[184:187], v240 offset:3136\n"
    "s_waitcnt lgkmcnt(11)\n"
    "v_mfma_f32_16x16x32_bf16 v[224:227], v[160:163], v[216:219], v[224:227]\n"
    "ds_read_b128 v[216:219], v241 offset:3456\n"
    "v_lshlrev_b32 v104, 16, v96\n"
    "s_waitcnt lgkmcnt(11)\n"
    "v_mfma_f32_16x16x32_bf16 v[228:231], v[180:183], v[192:195], 0\n"
    "v_and_b32 v105, 0xffff0000, v96\n"
    "s_waitcnt lgkmcnt(10)\n"
    "v_mfma_f32_16x16x32_bf16 v[228:231], v[160:163], v[212:215], v[228:231]\n"
    "v_lshlrev_b32 v106, 16, v97\n"
    "s_waitcnt lgkmcnt(9)\n"
    "v_mfma_f32_16x16x32_bf16 v[232:235], v[168:171], v[192:195], 0\n"
    "v_and_b32 v107, 0xffff0000, v97\n"
    "s_waitcnt lgkmcnt(8)\n"
    "v_mfma_f32_16x16x32_bf16 v[232:235], v[160:163], v[200:203], v[232:235]\n"
    "v_lshlrev_b32 v108, 16, v98\n"
    "s_waitcnt lgkmcnt(7)\n"
    "v_mfma_f32_16x16x32_bf16 v[236:239], v[164:167], v[192:195], 0\n"
    "ds_read_b128 v[192:195], v241 offset:384\n"
    "v_and_b32 v109, 0xffff0000, v98\n"
    "s_waitcnt lgkmcnt(7)\n"
    "v_mfma_f32_16x16x32_bf16 v[236:239], v[160:163], v[196:199], v[236:239]\n"
    "ds_read_b128 v[160:163], v240 offset:64\n"
    "v_lshlrev_b32 v110, 16, v99\n"
    "v_mfma_f32_16x16x32_bf16 v[224:227], v[180:183], v[196:199], v[224:227]\n"
    "ds_read_b128 v[180:183], v240 offset:2624\n"
    "v_and_b32 v111, 0xffff0000, v99\n"
    "v_mfma_f32_16x16x32_bf16 v[224:227], v[164:167], v[212:215], v[224:227]\n"
    "ds_read_b128 v[212:215], v241 offset:2944\n"
    "v_lshlrev_b32 v112, 16, v100\n"
    "v_mfma_f32_16x16x32_bf16 v[232:235], v[164:167], v[196:199], v[232:235]\n"
    "v_and_b32 v113, 0xffff0000, v100\n"
    "s_waitcnt lgkmcnt(9)\n"
    "v_mfma_f32_16x16x32_bf16 v[228:231], v[176:179], v[196:199], v[228:231]\n"
    "ds_read_b128 v[196:199], v241 offset:896\n"
    "v_lshlrev_b32 v114, 16, v101\n"
    "s_waitcnt lgkmcnt(9)\n"
    "v_mfma_f32_16x16x32_bf16 v[228:231], v[164:167], v[208:211], v[228:231]\n"
    "ds_read_b128 v[164:167], v240 offset:576\n"
    "v_and_b32 v115, 0xffff0000, v101\n"
    "v_mfma_f32_16x16x32_bf16 v[224:227], v[176:179], v[200:203], v[224:227]\n"
    "ds_read_b128 v[176:179], v240 offset:2112\n"
    "v_lshlrev_b32 v116, 16, v102\n"
    "v_mfma_f32_16x16x32_bf16 v[224:227], v[168:171], v[208:211], v[224:227]\n"
    "ds_read_b128 v[208:211], v241 offset:2432\n"
    "v_and_b32 v117, 0xffff0000, v102\n"
    "s_waitcnt lgkmcnt(11)\n"
    "v_mfma_f32_16x16x32_bf16 v[228:231], v[172:175], v[200:203], v[228:231]\n"
    "ds_read_b128 v[200:203], v241 offset:1408\n"
    "v_lshlrev_b32 v118, 16, v103\n"
    "s_waitcnt lgkmcnt(11)\n"
    "v_mfma_f32_16x16x32_bf16 v[228:231], v[168:171], v[204:207], v[228:231]\n"
    "ds_read_b128 v[168:171], v240 offset:1088\n"
    "v_and_b32 v119, 0xffff0000, v103\n"
    "v_mfma_f32_16x16x32_bf16 v[224:227], v[172:175], v[204:207], v[224:227]\n"
    "ds_read_b128 v[172:175], v240 offset:1600\n"
    "ds_read_b128 v[204:207], v241 offset:1920\n"
    "s_waitcnt lgkmcnt(11)\n"
    "v_mfma_f32_16x16x32_bf16 v[224:227], v[184:187], v[192:195], v[224:227]\n"
    "ds_read_b128 v[184:187], v240 offset:3200\n"
    "s_waitcnt lgkmcnt(11)\n"
    "v_mfma_f32_16x16x32_bf16 v[224:227], v[160:163], v[216:219], v[224:227]\n"
    "ds_read_b128 v[216:219], v241 offset:3392\n"
    "s_waitcnt lgkmcnt(11)\n"
    "v_mfma_f32_16x16x32_bf16 v[228:231], v[180:183], v[192:195], v[228:231]\n"
    "ds_write_b32 v27, v28 offset:64\n"
    "s_waitcnt vmcnt(0)\n"
    "buffer_store_dwordx2 v[18:19], v10, s[16:19], s69 offen offset:128 sc1\n"
    "s_waitcnt lgkmcnt(11)\n"
    "v_mfma_f32_16x16x32_bf16 v[228:231], v[160:163], v[212:215], v[228:231]\n"
    "s_waitcnt lgkmcnt(5)\n"
    "v_mfma_f32_16x16x32_bf16 v[232:235], v[168:171], v[192:195], v[232:235]\n"
    "v_mfma_f32_16x16x32_bf16 v[232:235], v[160:163], v[200:203], v[232:235]\n"
    "v_mfma_f32_16x16x32_bf16 v[236:239], v[164:167], v[192:195], v[236:239]\n"
    "ds_read_b128 v[192:195], v241 offset:320\n"
    "v_mfma_f32_16x16x32_bf16 v[236:239], v[160:163], v[196:199], v[236:239]\n"
    "ds_read_b128 v[160:163], v240 offset:128\n"
    "v_mfma_f32_16x16x32_bf16 v[224:227], v[180:183], v[196:199], v[224:227]\n"
    "ds_read_b128 v[180:183], v240 offset:2688\n"
    "v_mfma_f32_16x16x32_bf16 v[224:227], v[164:167], v[212:215], v[224:227]\n"
    "ds_read_b128 v[212:215], v241 offset:2880\n"
    "v_mfma_f32_16x16x32_bf16 v[232:235], v[164:167], v[196:199], v[232:235]\n"
    "v_mfma_f32_16x16x32_bf16 v[228:231], v[176:179], v[196:199], v[228:231]\n"
    "ds_read_b128 v[196:199], v241 offset:832\n"
    "v_mfma_f32_16x16x32_bf16 v[228:231], v[164:167], v[208:211], v[228:231]\n"
    "ds_read_b128 v[164:167], v240 offset:640\n"
    "v_mfma_f32_16x16x32_bf16 v[224:227], v[176:179], v[200:203], v[224:227]\n"
    "ds_read_b128 v[176:179], v240 offset:2176\n"
    "v_mfma_f32_16x16x32_bf16 v[224:227], v[168:171], v[208:211], v[224:227]\n"
    "ds_read_b128 v[208:211], v241 offset:2368\n"
    "s_waitcnt lgkmcnt(12)\n"
    "v_mfma_f32_16x16x32_bf16 v[228:231], v[172:175], v[200:203], v[228:231]\n"
    "ds_read_b128 v[200:203], v241 offset:1344\n"
    "s_waitcnt lgkmcnt(12)\n"
    "v_mfma_f32_16x16x32_bf16 v[228:231], v[168:171], v[204:207], v[228:231]\n"
    "ds_read_b128 v[168:171], v240 offset:1152\n"
    "v_mfma_f32_16x16x32_bf16 v[224:227], v[172:175], v[204:207], v[224:227]\n"
    "ds_read_b128 v[172:175], v240 offset:1664\n"
    "ds_read_b128 v[204:207], v241 offset:1856\n"
    "s_waitcnt lgkmcnt(11)\n"
    "v_mfma_f32_16x16x32_bf16 v[224:227], v[184:187], v[192:195], v[224:227]\n"
    "ds_read_b128 v[184:187], v240 offset:3264\n"
    "s_waitcnt lgkmcnt(11)\n"
    "v_mfma_f32_16x16x32_bf16 v[224:227], v[160:163], v[216:219], v[224:227]\n"
    "ds_read_b128 v[216:219], v241 offset:3328\n"
    "s_waitcnt lgkmcnt(11)\n"
    "v_mfma_f32_16x16x32_bf16 v[228:231], v[180:183], v[192:195], v[228:231]\n"
    "s_waitcnt lgkmcnt(10)\n"
    "v_mfma_f32_16x16x32_bf16 v[228:231], v[160:163], v[212:215], v[228:231]\n"
    "s_waitcnt lgkmcnt(4)\n"
    "v_mfma_f32_16x16x32_bf16 v[232:235], v[168:171], v[192:195], v[232:235]\n"
    "v_mfma_f32_16x16x32_bf16 v[232:235], v[160:163], v[200:203], v[232:235]\n"
    "v_mfma_f32_16x16x32_bf16 v[236:239], v[164:167], v[192:195], v[236:239]\n"
    "ds_read_b128 v[192:195], v241 offset:256\n"
    "v_mfma_f32_16x16x32_bf16 v[236:239], v[160:163], v[196:199], v[236:239]\n"
    "ds_read_b128 v[160:163], v240 offset:192\n"
    "v_mfma_f32_16x16x32_bf16 v[224:227], v[180:183], v[196:199], v[224:227]\n"
    "ds_read_b128 v[180:183], v240 offset:2752\n"
    "v_mfma_f32_16x16x32_bf16 v[224:227], v[164:167], v[212:215], v[224:227]\n"
    "ds_read_b128 v[212:215], v241 offset:2816\n"
    "v_mfma_f32_16x16x32_bf16 v[232:235], v[164:167], v[196:199], v[232:235]\n"
    "v_mfma_f32_16x16x32_bf16 v[228:231], v[176:179], v[196:199], v[228:231]\n"
    "ds_read_b128 v[196:199], v241 offset:768\n"
    "v_mfma_f32_16x16x32_bf16 v[228:231], v[164:167], v[208:211], v[228:231]\n"
    "ds_read_b128 v[164:167], v240 offset:704\n"
    "v_mfma_f32_16x16x32_bf16 v[224:227], v[176:179], v[200:203], v[224:227]\n"
    "ds_read_b128 v[176:179], v240 offset:2240\n"
    "v_mfma_f32_16x16x32_bf16 v[224:227], v[168:171], v[208:211], v[224:227]\n"
    "ds_read_b128 v[208:211], v241 offset:2304\n"
    "s_waitcnt lgkmcnt(11)\n"
    "v_mfma_f32_16x16x32_bf16 v[228:231], v[172:175], v[200:203], v[228:231]\n"
    "ds_read_b128 v[200:203], v241 offset:1280\n"
    "s_waitcnt lgkmcnt(11)\n"
    "v_mfma_f32_16x16x32_bf16 v[228:231], v[168:171], v[204:207], v[228:231]\n"
    "ds_read_b128 v[168:171], v240 offset:1216\n"
    "v_mfma_f32_16x16x32_bf16 v[224:227], v[172:175], v[204:207], v[224:227]\n"
    "ds_read_b128 v[172:175], v240 offset:1728\n"
    "ds_read_b128 v[204:207], v241 offset:1792\n"
    "s_waitcnt lgkmcnt(11)\n"
    "v_mfma_f32_16x16x32_bf16 v[224:227], v[184:187], v[192:195], v[224:227]\n"
    "ds_read_b128 v[184:187], v240 offset:3328\n"
    "s_waitcnt lgkmcnt(11)\n"
    "v_mfma_f32_16x16x32_bf16 v[224:227], v[160:163], v[216:219], v[224:227]\n"
    "ds_read_b128 v[216:219], v241 offset:3264\n"
    "s_waitcnt lgkmcnt(11)\n"
    "v_mfma_f32_16x16x32_bf16 v[228:231], v[180:183], v[192:195], v[228:231]\n"
    "s_waitcnt lgkmcnt(10)\n"
    "v_mfma_f32_16x16x32_bf16 v[228:231], v[160:163], v[212:215], v[228:231]\n"
    "s_waitcnt lgkmcnt(4)\n"
    "v_mfma_f32_16x16x32_bf16 v[232:235], v[168:171], v[192:195], v[232:235]\n"
    "v_mfma_f32_16x16x32_bf16 v[232:235], v[160:163], v[200:203], v[232:235]\n"
    "v_mfma_f32_16x16x32_bf16 v[236:239], v[164:167], v[192:195], v[236:239]\n"
    "ds_read_b128 v[192:195], v241 offset:192\n"
    "v_mfma_f32_16x16x32_bf16 v[236:239], v[160:163], v[196:199], v[236:239]\n"
    "ds_read_b128 v[160:163], v240 offset:256\n"
    "v_mfma_f32_16x16x32_bf16 v[224:227], v[180:183], v[196:199], v[224:227]\n"
    "ds_read_b128 v[180:183], v240 offset:2816\n"
    "v_mfma_f32_16x16x32_bf16 v[224:227], v[164:167], v[212:215], v[224:227]\n"
    "ds_read_b128 v[212:215], v241 offset:2752\n"
    "v_mfma_f32_16x16x32_bf16 v[232:235], v[164:167], v[196:199], v[232:235]\n"
    "v_mfma_f32_16x16x32_bf16 v[228:231], v[176:179], v[196:199], v[228:231]\n"
    "ds_read_b128 v[196:199], v241 offset:704\n"
    "v_mfma_f32_16x16x32_bf16 v[228:231], v[164:167], v[208:211], v[228:231]\n"
    "ds_read_b128 v[164:167], v240 offset:768\n"
    "v_mfma_f32_16x16x32_bf16 v[224:227], v[176:179], v[200:203], v[224:227]\n"
    "ds_read_b128 v[176:179], v240 offset:2304\n"
    "v_mfma_f32_16x16x32_bf16 v[224:227], v[168:171], v[208:211], v[224:227]\n"
    "ds_read_b128 v[208:211], v241 offset:2240\n"
    "s_waitcnt lgkmcnt(11)\n"
    "v_mfma_f32_16x16x32_bf16 v[228:231], v[172:175], v[200:203], v[228:231]\n"
    "ds_read_b128 v[200:203], v241 offset:1216\n"
    "s_waitcnt lgkmcnt(11)\n"
    "v_mfma_f32_16x16x32_bf16 v[228:231], v[168:171], v[204:207], v[228:231]\n"
    "ds_read_b128 v[168:171], v240 offset:1280\n"
    "v_mfma_f32_16x16x32_bf16 v[224:227], v[172:175], v[204:207], v[224:227]\n"
    "ds_read_b128 v[172:175], v240 offset:1792\n"
    "ds_read_b128 v[204:207], v241 offset:1728\n"
    "s_waitcnt lgkmcnt(11)\n"
    "v_mfma_f32_16x16x32_bf16 v[224:227], v[184:187], v[192:195], v[224:227]\n"
    "ds_read_b128 v[184:187], v240 offset:3392\n"
    "s_waitcnt lgkmcnt(11)\n"
    "v_mfma_f32_16x16x32_bf16 v[224:227], v[160:163], v[216:219], v[224:227]\n"
    "ds_read_b128 v[216:219], v241 offset:3200\n"
    "s_waitcnt lgkmcnt(11)\n"
    "v_mfma_f32_16x16x32_bf16 v[228:231], v[180:183], v[192:195], v[228:231]\n"
    "s_waitcnt lgkmcnt(10)\n"
    "v_mfma_f32_16x16x32_bf16 v[228:231], v[160:163], v[212:215], v[228:231]\n"
    "s_waitcnt lgkmcnt(4)\n"
    "v_mfma_f32_16x16x32_bf16 v[232:235], v[168:171], v[192:195], v[232:235]\n"
    "v_mfma_f32_16x16x32_bf16 v[232:235], v[160:163], v[200:203], v[232:235]\n"
    "v_mfma_f32_16x16x32_bf16 v[236:239], v[164:167], v[192:195], v[236:239]\n"
    "ds_read_b128 v[192:195], v241 offset:128\n"
    "v_mfma_f32_16x16x32_bf16 v[236:239], v[160:163], v[196:199], v[236:239]\n"
    "ds_read_b128 v[160:163], v240 offset:320\n"
    "v_mfma_f32_16x16x32_bf16 v[224:227], v[180:183], v[196:199], v[224:227]\n"
    "ds_read_b128 v[180:183], v240 offset:2880\n"
    "v_mfma_f32_16x16x32_bf16 v[224:227], v[164:167], v[212:215], v[224:227]\n"
    "ds_read_b128 v[212:215], v241 offset:2688\n"
    "v_mfma_f32_16x16x32_bf16 v[232:235], v[164:167], v[196:199], v[232:235]\n"
    "v_mfma_f32_16x16x32_bf16 v[228:231], v[176:179], v[196:199], v[228:231]\n"
    "ds_read_b128 v[196:199], v241 offset:640\n"
    "v_mfma_f32_16x16x32_bf16 v[228:231], v[164:167], v[208:211], v[228:231]\n"
    "ds_read_b128 v[164:167], v240 offset:832\n"
    "v_mfma_f32_16x16x32_bf16 v[224:227], v[176:179], v[200:203], v[224:227]\n"
    "ds_read_b128 v[176:179], v240 offset:2368\n"
    "v_mfma_f32_16x16x32_bf16 v[224:227], v[168:171], v[208:211], v[224:227]\n"
    "ds_read_b128 v[208:211], v241 offset:2176\n"
    "s_waitcnt lgkmcnt(11)\n"
    "v_mfma_f32_16x16x32_bf16 v[228:231], v[172:175], v[200:203], v[228:231]\n"
    "ds_read_b128 v[200:203], v241 offset:1152\n"
    "s_waitcnt lgkmcnt(11)\n"
    "v_mfma_f32_16x16x32_bf16 v[228:231], v[168:171], v[204:207], v[228:231]\n"
    "ds_read_b128 v[168:171], v240 offset:1344\n"
    "v_mfma_f32_16x16x32_bf16 v[224:227], v[172:175], v[204:207], v[224:227]\n"
    "ds_read_b128 v[172:175], v240 offset:1856\n"
    "ds_read_b128 v[204:207], v241 offset:1664\n"
    "s_waitcnt lgkmcnt(11)\n"
    "v_mfma_f32_16x16x32_bf16 v[224:227], v[184:187], v[192:195], v[224:227]\n"
    "ds_read_b128 v[184:187], v240 offset:3456\n"
    "s_waitcnt lgkmcnt(11)\n"
    "v_mfma_f32_16x16x32_bf16 v[224:227], v[160:163], v[216:219], v[224:227]\n"
    "ds_read_b128 v[216:219], v241 offset:3136\n"
    "s_waitcnt lgkmcnt(11)\n"
    "v_mfma_f32_16x16x32_bf16 v[228:231], v[180:183], v[192:195], v[228:231]\n"
    "s_waitcnt lgkmcnt(10)\n"
    "v_mfma_f32_16x16x32_bf16 v[228:231], v[160:163], v[212:215], v[228:231]\n"
    "s_waitcnt lgkmcnt(4)\n"
    "v_mfma_f32_16x16x32_bf16 v[232:235], v[168:171], v[192:195], v[232:235]\n"
    "v_mfma_f32_16x16x32_bf16 v[232:235], v[160:163], v[200:203], v[232:235]\n"
    "v_mfma_f32_16x16x32_bf16 v[236:239], v[164:167], v[192:195], v[236:239]\n"
    "ds_read_b128 v[192:195], v241 offset:64\n"
    "v_mfma_f32_16x16x32_bf16 v[236:239], v[160:163], v[196:199], v[236:239]\n"
    "ds_read_b128 v[160:163], v240 offset:384\n"
    "v_mfma_f32_16x16x32_bf16 v[224:227], v[180:183], v[196:199], v[224:227]\n"
    "ds_read_b128 v[180:183], v240 offset:2944\n"
    "v_mfma_f32_16x16x32_bf16 v[224:227], v[164:167], v[212:215], v[224:227]\n"
    "ds_read_b128 v[212:215], v241 offset:2624\n"
    "v_mfma_f32_16x16x32_bf16 v[232:235], v[164:167], v[196:199], v[232:235]\n"
    "v_mfma_f32_16x16x32_bf16 v[228:231], v[176:179], v[196:199], v[228:231]\n"
    "ds_read_b128 v[196:199], v241 offset:576\n"
    "v_mfma_f32_16x16x32_bf16 v[228:231], v[164:167], v[208:211], v[228:231]\n"
    "ds_read_b128 v[164:167], v240 offset:896\n"
    "v_mfma_f32_16x16x32_bf16 v[224:227], v[176:179], v[200:203], v[224:227]\n"
    "ds_read_b128 v[176:179], v240 offset:2432\n"
    "v_mfma_f32_16x16x32_bf16 v[224:227], v[168:171], v[208:211], v[224:227]\n"
    "ds_read_b128 v[208:211], v241 offset:2112\n"
    "s_waitcnt lgkmcnt(11)\n"
    "v_mfma_f32_16x16x32_bf16 v[228:231], v[172:175], v[200:203], v[228:231]\n"
    "ds_read_b128 v[200:203], v241 offset:1088\n"
    "s_waitcnt lgkmcnt(11)\n"
    "v_mfma_f32_16x16x32_bf16 v[228:231], v[168:171], v[204:207], v[228:231]\n"
    "ds_read_b128 v[168:171], v240 offset:1408\n"
    "v_mfma_f32_16x16x32_bf16 v[224:227], v[172:175], v[204:207], v[224:227]\n"
    "ds_read_b128 v[172:175], v240 offset:1920\n"
    "ds_read_b128 v[204:207], v241 offset:1600\n"
    "s_waitcnt lgkmcnt(11)\n"
    "v_mfma_f32_16x16x32_bf16 v[224:227], v[184:187], v[192:195], v[224:227]\n"
    "ds_read_b128 v[184:187], v240 offset:3520\n"
    "s_waitcnt lgkmcnt(11)\n"
    "v_mfma_f32_16x16x32_bf16 v[224:227], v[160:163], v[216:219], v[224:227]\n"
    "ds_read_b128 v[216:219], v241 offset:3072\n"
    "s_waitcnt lgkmcnt(11)\n"
    "v_mfma_f32_16x16x32_bf16 v[228:231], v[180:183], v[192:195], v[228:231]\n"
    "s_waitcnt lgkmcnt(10)\n"
    "v_mfma_f32_16x16x32_bf16 v[228:231], v[160:163], v[212:215], v[228:231]\n"
    "s_waitcnt lgkmcnt(4)\n"
    "v_mfma_f32_16x16x32_bf16 v[232:235], v[168:171], v[192:195], v[232:235]\n"
    "v_mfma_f32_16x16x32_bf16 v[232:235], v[160:163], v[200:203], v[232:235]\n"
    "v_mfma_f32_16x16x32_bf16 v[236:239], v[164:167], v[192:195], v[236:239]\n"
    "ds_read_b128 v[192:195], v241 offset:0\n"
    "v_mfma_f32_16x16x32_bf16 v[236:239], v[160:163], v[196:199], v[236:239]\n"
    "ds_read_b128 v[160:163], v240 offset:448\n"
    "v_mfma_f32_16x16x32_bf16 v[224:227], v[180:183], v[196:199], v[224:227]\n"
    "ds_read_b128 v[180:183], v240 offset:3008\n"
    "v_mfma_f32_16x16x32_bf16 v[224:227], v[164:167], v[212:215], v[224:227]\n"
    "ds_read_b128 v[212:215], v241 offset:2560\n"
    "v_mfma_f32_16x16x32_bf16 v[232:235], v[164:167], v[196:199], v[232:235]\n"
    "v_mfma_f32_16x16x32_bf16 v[228:231], v[176:179], v[196:199], v[228:231]\n"
    "ds_read_b128 v[196:199], v241 offset:512\n"
    "v_mfma_f32_16x16x32_bf16 v[228:231], v[164:167], v[208:211], v[228:231]\n"
    "ds_read_b128 v[164:167], v240 offset:960\n"
    "v_mfma_f32_16x16x32_bf16 v[224:227], v[176:179], v[200:203], v[224:227]\n"
    "ds_read_b128 v[176:179], v240 offset:2496\n"
    "v_mfma_f32_16x16x32_bf16 v[224:227], v[168:171], v[208:211], v[224:227]\n"
    "ds_read_b128 v[208:211], v241 offset:2048\n"
    "s_waitcnt lgkmcnt(11)\n"
    "v_mfma_f32_16x16x32_bf16 v[228:231], v[172:175], v[200:203], v[228:231]\n"
    "ds_read_b128 v[200:203], v241 offset:1024\n"
    "s_waitcnt lgkmcnt(11)\n"
    "v_mfma_f32_16x16x32_bf16 v[228:231], v[168:171], v[204:207], v[228:231]\n"
    "ds_read_b128 v[168:171], v240 offset:1472\n"
    "v_mfma_f32_16x16x32_bf16 v[224:227], v[172:175], v[204:207], v[224:227]\n"
    "ds_read_b128 v[172:175], v240 offset:1984\n"
    "ds_read_b128 v[204:207], v241 offset:1536\n"
    "s_waitcnt lgkmcnt(11)\n"
    "v_mfma_f32_16x16x32_bf16 v[224:227], v[184:187], v[192:195], v[224:227]\n"
    "s_waitcnt lgkmcnt(10)\n"
    "v_mfma_f32_16x16x32_bf16 v[224:227], v[160:163], v[216:219], v[224:227]\n"
    "s_waitcnt lgkmcnt(9)\n"
    "v_mfma_f32_16x16x32_bf16 v[228:231], v[180:183], v[192:195], v[228:231]\n"
    "s_waitcnt lgkmcnt(8)\n"
    "v_mfma_f32_16x16x32_bf16 v[228:231], v[160:163], v[212:215], v[228:231]\n"
    "s_waitcnt lgkmcnt(2)\n"
    "v_mfma_f32_16x16x32_bf16 v[232:235], v[168:171], v[192:195], v[232:235]\n"
    "v_mfma_f32_16x16x32_bf16 v[232:235], v[160:163], v[200:203], v[232:235]\n"
    "v_mfma_f32_16x16x32_bf16 v[236:239], v[164:167], v[192:195], v[236:239]\n"
    "v_mfma_f32_16x16x32_bf16 v[236:239], v[160:163], v[196:199], v[236:239]\n"
    "v_mfma_f32_16x16x32_bf16 v[224:227], v[180:183], v[196:199], v[224:227]\n"
    "v_mfma_f32_16x16x32_bf16 v[224:227], v[164:167], v[212:215], v[224:227]\n"
    "v_mfma_f32_16x16x32_bf16 v[232:235], v[164:167], v[196:199], v[232:235]\n"
    "v_mfma_f32_16x16x32_bf16 v[228:231], v[176:179], v[196:199], v[228:231]\n"
    "v_mfma_f32_16x16x32_bf16 v[228:231], v[164:167], v[208:211], v[228:231]\n"
    "v_mfma_f32_16x16x32_bf16 v[224:227], v[176:179], v[200:203], v[224:227]\n"
    "v_mfma_f32_16x16x32_bf16 v[224:227], v[168:171], v[208:211], v[224:227]\n"
    "s_waitcnt lgkmcnt(1)\n"
    "v_mfma_f32_16x16x32_bf16 v[228:231], v[172:175], v[200:203], v[228:231]\n"
    "s_waitcnt lgkmcnt(0)\n"
    "v_mfma_f32_16x16x32_bf16 v[228:231], v[168:171], v[204:207], v[228:231]\n"
    "v_mfma_f32_16x16x32_bf16 v[224:227], v[172:175], v[204:207], v[224:227]\n"
    "s_setprio 0\n"
    "v_fma_f32 v116, v116, s98, v236\n"
    "v_fma_f32 v117, v117, s98, v237\n"
    "v_fma_f32 v118, v118, s98, v238\n"
    "v_fma_f32 v119, v119, s98, v239\n"
    "v_cvt_pk_bf16_f32 v102, v116, v117\n"
    "v_cvt_pk_bf16_f32 v103, v118, v119\n"
    "v_fma_f32 v112, v112, s98, v232\n"
    "v_fma_f32 v113, v113, s98, v233\n"
    "v_fma_f32 v114, v114, s98, v234\n"
    "v_fma_f32 v115, v115, s98, v235\n"
    "v_cvt_pk_bf16_f32 v100, v112, v113\n"
    "v_cvt_pk_bf16_f32 v101, v114, v115\n"
    "v_fma_f32 v108, v108, s98, v228\n"
    "v_fma_f32 v109, v109, s98, v229\n"
    "v_fma_f32 v110, v110, s98, v230\n"
    "v_fma_f32 v111, v111, s98, v231\n"
    "v_cvt_pk_bf16_f32 v98, v108, v109\n"
    "v_cvt_pk_bf16_f32 v99, v110, v111\n"
    "v_fma_f32 v104, v104, s98, v224\n"
    "v_fma_f32 v105, v105, s98, v225\n"
    "v_fma_f32 v106, v106, s98, v226\n"
    "v_fma_f32 v107, v107, s98, v227\n"
    "v_cvt_pk_bf16_f32 v96, v104, v105\n"
    "v_cvt_pk_bf16_f32 v97, v106, v107\n"
    "s_nop 0\n"
    "v_permlane32_swap_b32 v96, v100\n"
    "v_permlane32_swap_b32 v97, v101\n"
    "v_permlane32_swap_b32 v98, v102\n"
    "v_permlane32_swap_b32 v99, v103\n"
    "s_nop 0\n"
    "v_permlane16_swap_b32 v96, v98\n"
    "v_permlane16_swap_b32 v97, v99\n"
    "v_permlane16_swap_b32 v100, v102\n"
    "v_permlane16_swap_b32 v101, v103\n"
    "buffer_store_dwordx4 v[96:99], v242, s[16:19], s74 offen offset:0 sc1\n"
    "buffer_store_dwordx4 v[100:103], v242, s[16:19], s74 offen offset:64 sc1\n"
    "s_waitcnt lgkmcnt(0)\n"
    "Lcvd_%=:\n"
    "ds_write_b32 v27, v28 offset:48\n"
    "v_mov_b32 v255, v9\n"
    "v_xor_b32 v21, s63, v16\n"
    "v_lshlrev_b32 v21, 4, v21\n"
    "v_add_u32 v21, 153600, v21\n"
    "v_and_b32 v22, 0xf80, v3\n"
    "v_add_u32 v11, v22, v21\n"
    "v_lshrrev_b32 v21, 5, v1\n"
    "v_lshl_add_u32 v21, s65, 2, v21\n"
    "v_xor_b32 v21, v21, v16\n"
    "v_lshlrev_b32 v21, 4, v21\n"
    "v_lshl_add_u32 v21, v16, 10, v21\n"
    "v_bfe_u32 v23, v0, 3, 2\n"
    "v_lshl_add_u32 v21, v23, 1, v21\n"
    "s_lshl_b32 s77, s64, 3\n"
    "s_add_u32 s77, s77, 153600\n"
    "v_add_u32 v12, s77, v21\n"
    "v_lshrrev_b32 v23, 3, v1\n"
    "v_lshlrev_b32 v22, 1, v16\n"
    "v_xor_b32 v22, v22, v23\n"
    "v_lshlrev_b32 v22, 4, v22\n"
    "v_lshl_add_u32 v22, v23, 8, v22\n"
    "v_lshl_add_u32 v15, s63, 11, v22\n"
    "v_xor_b32 v30, 16, v15\n"
    "v_add_u32 v29, 98304, v15\n"
    "v_add_u32 v10, 98304, v30\n"
    "v_add_u32 v13, 49152, v15\n"
    "v_add_u32 v14, 49152, v30\n"
    "v_xor_b32 v224, 64, v11\n"
    "v_xor_b32 v225, 32, v12\n"
    "ds_read_b32 v26, v27 offset:32\n"
    "s_waitcnt lgkmcnt(0)\n"
    "v_cmp_ne_u32 vcc, 1, v26\n"
    "s_cbranch_vccnz Lsp11_%=\n"
    "Lsb11_%=:\n"
    "ds_read_b64_tr_b16 v[160:161], v7 offset:0\n"
    "ds_read_b64_tr_b16 v[162:163], v7 offset:2048\n"
    "ds_read_b64_tr_b16 v[164:165], v8 offset:0\n"
    "ds_read_b64_tr_b16 v[166:167], v8 offset:2048\n"
    "ds_read_b64_tr_b16 v[168:169], v7 offset:512\n"
    "ds_read_b64_tr_b16 v[170:171], v7 offset:2560\n"
    "ds_read_b64_tr_b16 v[172:173], v8 offset:512\n"
    "ds_read_b64_tr_b16 v[174:175], v8 offset:2560\n"
    "s_add_u32 s79, s70, s32\n"
    "s_add_u32 s79, s79, 0\n"
    "buffer_load_dwordx4 v[48:51], v2, s[16:19], s79 offen offset:0 sc1\n"
    "buffer_load_dwordx4 v[52:55], v2, s[16:19], s79 offen offset:1024 sc1\n"
    "buffer_load_dwordx4 v[56:59], v2, s[16:19], s79 offen offset:2048 sc1\n"
    "buffer_load_dwordx4 v[60:63], v2, s[16:19], s79 offen offset:3072 sc1\n"
    "s_add_u32 s79, s70, s32\n"
    "s_add_u32 s79, s79, 4096\n"
    "buffer_load_dwordx4 v[64:67], v2, s[16:19], s79 offen offset:0 sc1\n"
    "buffer_load_dwordx4 v[68:71], v2, s[16:19], s79 offen offset:1024 sc1\n"
    "buffer_load_dwordx4 v[72:75], v2, s[16:19], s79 offen offset:2048 sc1\n"
    "buffer_load_dwordx4 v[76:79], v2, s[16:19], s79 offen offset:3072 sc1\n"
    "s_add_u32 s79, s70, s33\n"
    "s_add_u32 s79, s79, 0\n"
    "buffer_load_dwordx4 v[80:83], v2, s[16:19], s79 offen offset:0 sc1\n"
    "buffer_load_dwordx4 v[84:87], v2, s[16:19], s79 offen offset:1024 sc1\n"
    "buffer_load_dwordx4 v[88:91], v2, s[16:19], s79 offen offset:2048 sc1\n"
    "buffer_load_dwordx4 v[92:95], v2, s[16:19], s79 offen offset:3072 sc1\n"
    "s_waitcnt vmcnt(12)\n"
    "buffer_store_dwordx2 v[18:19], v255, s[16:19], s75 offen offset:0 sc1\n"
    "s_waitcnt vmcnt(12) lgkmcnt(0)\n"
    "v_mfma_f32_16x16x32_bf16 v[96:99], v[160:163], v[48:51], 0\n"
    "s_add_u32 s79, s70, s33\n"
    "s_add_u32 s79, s79, 4096\n"
    "buffer_load_dwordx4 v[32:35], v2, s[16:19], s79 offen offset:0 sc1\n"
    "ds_read_b64_tr_b16 v[176:177], v7 offset:4096\n"
    "v_mfma_f32_16x16x32_bf16 v[112:115], v[164:167], v[48:51], 0\n"
    "ds_read_b64_tr_b16 v[178:179], v7 offset:6144\n"
    "v_mfma_f32_16x16x32_bf16 v[128:131], v[168:171], v[48:51], 0\n"
    "ds_read_b64_tr_b16 v[180:181], v8 offset:4096\n"
    "v_mfma_f32_16x16x32_bf16 v[144:147], v[172:175], v[48:51], 0\n"
    "ds_read_b64_tr_b16 v[182:183], v8 offset:6144\n"
    "s_waitcnt vmcnt(12)\n"
    "v_mfma_f32_16x16x32_bf16 v[100:103], v[160:163], v[52:55], 0\n"
    "ds_read_b64_tr_b16 v[184:185], v7 offset:4608\n"
    "buffer_load_dwordx4 v[36:39], v2, s[16:19], s79 offen offset:1024 sc1\n"
    "ds_read_b64_tr_b16 v[186:187], v7 offset:6656\n"
    "v_mfma_f32_16x16x32_bf16 v[116:119], v[164:167], v[52:55], 0\n"
    "ds_read_b64_tr_b16 v[188:189], v8 offset:4608\n"
    "v_mfma_f32_16x16x32_bf16 v[132:135], v[168:171], v[52:55], 0\n"
    "ds_read_b64_tr_b16 v[190:191], v8 offset:6656\n"
    "v_mfma_f32_16x16x32_bf16 v[148:151], v[172:175], v[52:55], 0\n"
    "s_waitcnt vmcnt(12)\n"
    "v_mfma_f32_16x16x32_bf16 v[104:107], v[160:163], v[56:59], 0\n"
    "buffer_load_dwordx4 v[40:43], v2, s[16:19], s79 offen offset:2048 sc1\n"
    "v_mfma_f32_16x16x32_bf16 v[120:123], v[164:167], v[56:59], 0\n"
    "v_mfma_f32_16x16x32_bf16 v[136:139], v[168:171], v[56:59], 0\n"
    "v_mfma_f32_16x16x32_bf16 v[152:155], v[172:175], v[56:59], 0\n"
    "s_waitcnt vmcnt(12)\n"
    "v_mfma_f32_16x16x32_bf16 v[108:111], v[160:163], v[60:63], 0\n"
    "buffer_load_dwordx4 v[44:47], v2, s[16:19], s79 offen offset:3072 sc1\n"
    "v_mfma_f32_16x16x32_bf16 v[124:127], v[164:167], v[60:63], 0\n"
    "v_mfma_f32_16x16x32_bf16 v[140:143], v[168:171], v[60:63], 0\n"
    "v_mfma_f32_16x16x32_bf16 v[156:159], v[172:175], v[60:63], 0\n"
    "s_waitcnt vmcnt(12) lgkmcnt(0)\n"
    "v_mfma_f32_16x16x32_bf16 v[96:99], v[176:179], v[64:67], v[96:99]\n"
    "v_mfma_f32_16x16x32_bf16 v[112:115], v[180:183], v[64:67], v[112:115]\n"
    "ds_read_b64_tr_b16 v[160:161], v7 offset:16384\n"
    "v_mfma_f32_16x16x32_bf16 v[128:131], v[184:187], v[64:67], v[128:131]\n"
    "ds_read_b64_tr_b16 v[162:163], v7 offset:18432\n"
    "v_mfma_f32_16x16x32_bf16 v[144:147], v[188:191], v[64:67], v[144:147]\n"
    "ds_read_b64_tr_b16 v[164:165], v8 offset:16384\n"
    "s_waitcnt vmcnt(11)\n"
    "v_mfma_f32_16x16x32_bf16 v[100:103], v[176:179], v[68:71], v[100:103]\n"
    "ds_read_b64_tr_b16 v[166:167], v8 offset:18432\n"
    "v_mfma_f32_16x16x32_bf16 v[116:119], v[180:183], v[68:71], v[116:119]\n"
    "ds_read_b64_tr_b16 v[168:169], v7 offset:16896\n"
    "v_mfma_f32_16x16x32_bf16 v[132:135], v[184:187], v[68:71], v[132:135]\n"
    "ds_read_b64_tr_b16 v[170:171], v7 offset:18944\n"
    "v_mfma_f32_16x16x32_bf16 v[148:151], v[188:191], v[68:71], v[148:151]\n"
    "ds_read_b64_tr_b16 v[172:173], v8 offset:16896\n"
    "s_waitcnt vmcnt(10)\n"
    "v_mfma_f32_16x16x32_bf16 v[104:107], v[176:179], v[72:75], v[104:107]\n"
    "ds_read_b64_tr_b16 v[174:175], v8 offset:18944\n"
    "v_mfma_f32_16x16x32_bf16 v[120:123], v[180:183], v[72:75], v[120:123]\n"
    "v_mfma_f32_16x16x32_bf16 v[136:139], v[184:187], v[72:75], v[136:139]\n"
    "v_mfma_f32_16x16x32_bf16 v[152:155], v[188:191], v[72:75], v[152:155]\n"
    "s_waitcnt vmcnt(9)\n"
    "v_mfma_f32_16x16x32_bf16 v[108:111], v[176:179], v[76:79], v[108:111]\n"
    "v_mfma_f32_16x16x32_bf16 v[124:127], v[180:183], v[76:79], v[124:127]\n"
    "v_mfma_f32_16x16x32_bf16 v[140:143], v[184:187], v[76:79], v[140:143]\n"
    "v_mfma_f32_16x16x32_bf16 v[156:159], v[188:191], v[76:79], v[156:159]\n"
    "buffer_load_dwordx4 v[48:51], v250, s[4:7], 0 offen\n"
    "s_mov_b32 s76, 0x8000\n"
    "buffer_load_dwordx4 v[52:55], v250, s[4:7], s76 offen\n"
    "buffer_load_dwordx4 v[240:243], v254, s[44:47], 0 offen\n"
    "buffer_load_dwordx4 v[56:59], v253, s[16:19], 0 offen offset:0 sc1\n"
    "buffer_load_dwordx4 v[60:63], v253, s[16:19], 0 offen offset:512 sc1\n"
    "s_waitcnt vmcnt(13) lgkmcnt(0)\n"
    "v_mfma_f32_16x16x32_bf16 v[96:99], v[160:163], v[80:83], v[96:99]\n"
    "v_mfma_f32_16x16x32_bf16 v[112:115], v[164:167], v[80:83], v[112:115]\n"
    "ds_read_b64_tr_b16 v[176:177], v7 offset:20480\n"
    "v_mfma_f32_16x16x32_bf16 v[128:131], v[168:171], v[80:83], v[128:131]\n"
    "ds_read_b64_tr_b16 v[178:179], v7 offset:22528\n"
    "v_mfma_f32_16x16x32_bf16 v[144:147], v[172:175], v[80:83], v[144:147]\n"
    "ds_read_b64_tr_b16 v[180:181], v8 offset:20480\n"
    "s_waitcnt vmcnt(12)\n"
    "v_mfma_f32_16x16x32_bf16 v[100:103], v[160:163], v[84:87], v[100:103]\n"
    "ds_read_b64_tr_b16 v[182:183], v8 offset:22528\n"
    "v_mfma_f32_16x16x32_bf16 v[116:119], v[164:167], v[84:87], v[116:119]\n"
    "ds_read_b64_tr_b16 v[184:185], v7 offset:20992\n"
    "v_mfma_f32_16x16x32_bf16 v[132:135], v[168:171], v[84:87], v[132:135]\n"
    "ds_read_b64_tr_b16 v[186:187], v7 offset:23040\n"
    "v_mfma_f32_16x16x32_bf16 v[148:151], v[172:175], v[84:87], v[148:151]\n"
    "ds_read_b64_tr_b16 v[188:189], v8 offset:20992\n"
    "s_waitcnt vmcnt(11)\n"
    "v_mfma_f32_16x16x32_bf16 v[104:107], v[160:163], v[88:91], v[104:107]\n"
    "ds_read_b64_tr_b16 v[190:191], v8 offset:23040\n"
    "v_mfma_f32_16x16x32_bf16 v[120:123], v[164:167], v[88:91], v[120:123]\n"
    "v_mfma_f32_16x16x32_bf16 v[136:139], v[168:171], v[88:91], v[136:139]\n"
    "v_mfma_f32_16x16x32_bf16 v[152:155], v[172:175], v[88:91], v[152:155]\n"
    "s_waitcnt vmcnt(10)\n"
    "v_mfma_f32_16x16x32_bf16 v[108:111], v[160:163], v[92:95], v[108:111]\n"
    "v_mfma_f32_16x16x32_bf16 v[124:127], v[164:167], v[92:95], v[124:127]\n"
    "v_mfma_f32_16x16x32_bf16 v[140:143], v[168:171], v[92:95], v[140:143]\n"
    "v_mfma_f32_16x16x32_bf16 v[156:159], v[172:175], v[92:95], v[156:159]\n"
    "s_waitcnt vmcnt(8) lgkmcnt(0)\n"
    "v_mfma_f32_16x16x32_bf16 v[96:99], v[176:179], v[32:35], v[96:99]\n"
    "v_mfma_f32_16x16x32_bf16 v[112:115], v[180:183], v[32:35], v[112:115]\n"
    "v_mfma_f32_16x16x32_bf16 v[128:131], v[184:187], v[32:35], v[128:131]\n"
    "v_mfma_f32_16x16x32_bf16 v[144:147], v[188:191], v[32:35], v[144:147]\n"
    "s_waitcnt vmcnt(7)\n"
    "v_mfma_f32_16x16x32_bf16 v[100:103], v[176:179], v[36:39], v[100:103]\n"
    "v_mfma_f32_16x16x32_bf16 v[116:119], v[180:183], v[36:39], v[116:119]\n"
    "v_mfma_f32_16x16x32_bf16 v[132:135], v[184:187], v[36:39], v[132:135]\n"
    "s_nop 0\n"
    "ds_write_b128 v6, v[96:99] offset:0\n"
    "v_mfma_f32_16x16x32_bf16 v[148:151], v[188:191], v[36:39], v[148:151]\n"
    "ds_write_b128 v20, v[112:115] offset:0\n"
    "s_waitcnt vmcnt(6)\n"
    "v_mfma_f32_16x16x32_bf16 v[104:107], v[176:179], v[40:43], v[104:107]\n"
    "ds_write_b128 v6, v[128:131] offset:128\n"
    "v_mfma_f32_16x16x32_bf16 v[120:123], v[180:183], v[40:43], v[120:123]\n"
    "ds_write_b128 v20, v[144:147] offset:128\n"
    "v_mfma_f32_16x16x32_bf16 v[136:139], v[184:187], v[40:43], v[136:139]\n"
    "ds_write_b128 v6, v[100:103] offset:4096\n"
    "v_mfma_f32_16x16x32_bf16 v[152:155], v[188:191], v[40:43], v[152:155]\n"
    "ds_write_b128 v20, v[116:119] offset:4096\n"
    "s_waitcnt vmcnt(5)\n"
    "v_mfma_f32_16x16x32_bf16 v[108:111], v[176:179], v[44:47], v[108:111]\n"
    "ds_write_b128 v6, v[132:135] offset:4224\n"
    "v_mfma_f32_16x16x32_bf16 v[124:127], v[180:183], v[44:47], v[124:127]\n"
    "ds_write_b128 v20, v[148:151] offset:4224\n"
    "v_mfma_f32_16x16x32_bf16 v[140:143], v[184:187], v[44:47], v[140:143]\n"
    "ds_write_b128 v24, v[104:107] offset:0\n"
    "v_mfma_f32_16x16x32_bf16 v[156:159], v[188:191], v[44:47], v[156:159]\n"
    "ds_write_b128 v25, v[120:123] offset:0\n"
    "ds_write_b128 v24, v[136:139] offset:128\n"
    "ds_write_b128 v25, v[152:155] offset:128\n"
    "ds_write_b128 v24, v[108:111] offset:4096\n"
    "ds_write_b128 v25, v[124:127] offset:4096\n"
    "s_nop 0\n"
    "ds_write_b128 v24, v[140:143] offset:4224\n"
    "s_nop 0\n"
    "ds_write_b128 v25, v[156:159] offset:4224\n"
    "buffer_load_dwordx2 v[228:229], v251, s[16:19], 0 offen sc1\n"
    "ds_write_b32 v27, v28 offset:80\n"
    "s_waitcnt vmcnt(1)\n"
    "v_cmp_ne_u32 vcc, s52, v57\n"
    "v_cmp_ne_u32 s[100:101], s53, v59\n"
    "s_or_b64 vcc, vcc, s[100:101]\n"
    "s_cbranch_scc1 Lsp9_%=\n"
    "Lsb9_%=:\n"
    "v_cmp_ne_u32 vcc, s52, v61\n"
    "v_cmp_ne_u32 s[100:101], s53, v63\n"
    "s_or_b64 vcc, vcc, s[100:101]\n"
    "s_cbranch_scc1 Lsp10_%=\n"
    "Lsb10_%=:\n"
    "v_add_f32 v58, 0x3727c5ac, v58\n"
    "v_rsq_f32 v58, v58\n"
    "v_lshlrev_b32 v192, 16, v48\n"
    "v_and_b32 v193, 0xffff0000, v48\n"
    "v_lshlrev_b32 v194, 16, v49\n"
    "v_and_b32 v195, 0xffff0000, v49\n"
    "v_lshlrev_b32 v196, 16, v50\n"
    "v_and_b32 v197, 0xffff0000, v50\n"
    "v_lshlrev_b32 v198, 16, v51\n"
    "v_and_b32 v199, 0xffff0000, v51\n"
    "v_mul_f32 v56, v56, v58\n"
    "v_fma_f32 v208, v192, v58, v56\n"
    "v_fma_f32 v209, v193, v58, v56\n"
    "v_fma_f32 v210, v194, v58, v56\n"
    "v_fma_f32 v211, v195, v58, v56\n"
    "v_fma_f32 v212, v196, v58, v56\n"
    "v_fma_f32 v213, v197, v58, v56\n"
    "v_fma_f32 v214, v198, v58, v56\n"
    "v_fma_f32 v215, v199, v58, v56\n"
    "v_add_f32 v62, 0x3727c5ac, v62\n"
    "v_rsq_f32 v62, v62\n"
    "v_lshlrev_b32 v200, 16, v52\n"
    "v_and_b32 v201, 0xffff0000, v52\n"
    "v_lshlrev_b32 v202, 16, v53\n"
    "v_and_b32 v203, 0xffff0000, v53\n"
    "v_lshlrev_b32 v204, 16, v54\n"
    "v_and_b32 v205, 0xffff0000, v54\n"
    "v_lshlrev_b32 v206, 16, v55\n"
    "v_and_b32 v207, 0xffff0000, v55\n"
    "v_mul_f32 v60, v60, v62\n"
    "v_fma_f32 v216, v200, v62, v60\n"
    "v_fma_f32 v217, v201, v62, v60\n"
    "v_fma_f32 v218, v202, v62, v60\n"
    "v_fma_f32 v219, v203, v62, v60\n"
    "v_fma_f32 v220, v204, v62, v60\n"
    "v_fma_f32 v221, v205, v62, v60\n"
    "v_fma_f32 v222, v206, v62, v60\n"
    "v_fma_f32 v223, v207, v62, v60\n"
    "v_lshlrev_b32 v48, 16, v240\n"
    "v_and_b32 v49, 0xffff0000, v240\n"
    "v_lshlrev_b32 v50, 16, v241\n"
    "v_and_b32 v51, 0xffff0000, v241\n"
    "v_lshlrev_b32 v52, 16, v242\n"
    "v_and_b32 v53, 0xffff0000, v242\n"
    "v_lshlrev_b32 v54, 16, v243\n"
    "v_and_b32 v55, 0xffff0000, v243\n"
    "v_add_f32 v192, v192, v48\n"
    "v_add_f32 v193, v193, v49\n"
    "v_add_f32 v194, v194, v50\n"
    "v_add_f32 v195, v195, v51\n"
    "v_add_f32 v196, v196, v52\n"
    "v_add_f32 v197, v197, v53\n"
    "v_add_f32 v198, v198, v54\n"
    "v_add_f32 v199, v199, v55\n"
    "v_add_f32 v200, v200, v48\n"
    "v_add_f32 v201, v201, v49\n"
    "v_add_f32 v202, v202, v50\n"
    "v_add_f32 v203, v203, v51\n"
    "v_add_f32 v204, v204, v52\n"
    "v_add_f32 v205, v205, v53\n"
    "v_add_f32 v206, v206, v54\n"
    "v_add_f32 v207, v207, v55\n"
    "ds_read_b32 v26, v31 offset:80\n"
    "v_mov_b32 v64, 0\n"
    "v_mov_b32 v65, 0\n"
    "v_mov_b32 v66, 0\n"
    "v_mov_b32 v67, 0\n"
    "v_mov_b32 v68, 0\n"
    "v_mov_b32 v69, 0\n"
    "v_mov_b32 v70, 0\n"
    "v_mov_b32 v71, 0\n"
    "v_mov_b32 v72, 0\n"
    "v_mov_b32 v73, 0\n"
    "v_mov_b32 v74, 0\n"
    "v_mov_b32 v75, 0\n"
    "v_mov_b32 v76, 0\n"
    "v_mov_b32 v77, 0\n"
    "v_mov_b32 v78, 0\n"
    "v_mov_b32 v79, 0\n"
    "s_waitcnt lgkmcnt(0)\n"
    "v_cmp_ne_u32 vcc, 1, v26\n"
    "s_cbranch_vccnz Lsp12_%=\n"
    "Lsb12_%=:\n"
    "ds_read_b128 v[96:99], v15 offset:0\n"
    "ds_read_b128 v[100:103], v30 offset:0\n"
    "ds_read_b128 v[104:107], v15 offset:8192\n"
    "ds_read_b128 v[108:111], v30 offset:8192\n"
    "ds_read_b128 v[112:115], v13 offset:0\n"
    "ds_read_b128 v[116:119], v14 offset:0\n"
    "ds_read_b128 v[120:123], v13 offset:8192\n"
    "ds_read_b128 v[124:127], v14 offset:8192\n"
    "ds_read_b128 v[128:131], v15 offset:16384\n"
    "ds_read_b128 v[132:135], v30 offset:16384\n"
    "ds_read_b128 v[136:139], v15 offset:24576\n"
    "ds_read_b128 v[140:143], v30 offset:24576\n"
    "ds_read_b128 v[144:147], v13 offset:16384\n"
    "ds_read_b128 v[148:151], v14 offset:16384\n"
    "ds_read_b128 v[152:155], v13 offset:24576\n"
    "ds_read_b128 v[156:159], v14 offset:24576\n"
    "s_waitcnt vmcnt(0)\n"
    "v_cmp_ne_u64 vcc, s[52:53], v[228:229]\n"
    "s_cbranch_vccnz Lzn_%=\n"
    "buffer_load_dwordx4 v[232:235], v252, s[16:19], 0 offen sc1\n"
    "s_mov_b32 s79, 0x20000\n"
    "buffer_load_dwordx4 v[236:239], v252, s[16:19], s79 offen sc1\n"
    "Lzn_%=:\n"
    "s_waitcnt lgkmcnt(14)\n"
    "v_pk_add_f32 v[192:193], v[192:193], v[96:97]\n"
    "v_pk_add_f32 v[194:195], v[194:195], v[98:99]\n"
    "v_pk_add_f32 v[196:197], v[196:197], v[100:101]\n"
    "v_pk_add_f32 v[198:199], v[198:199], v[102:103]\n"
    "s_waitcnt lgkmcnt(12)\n"
    "v_pk_add_f32 v[192:193], v[192:193], v[104:105]\n"
    "v_pk_add_f32 v[194:195], v[194:195], v[106:107]\n"
    "v_pk_add_f32 v[196:197], v[196:197], v[108:109]\n"
    "v_pk_add_f32 v[198:199], v[198:199], v[110:111]\n"
    "s_waitcnt lgkmcnt(10)\n"
    "v_pk_add_f32 v[192:193], v[192:193], v[112:113]\n"
    "v_pk_add_f32 v[194:195], v[194:195], v[114:115]\n"
    "v_pk_add_f32 v[196:197], v[196:197], v[116:117]\n"
    "v_pk_add_f32 v[198:199], v[198:199], v[118:119]\n"
    "s_waitcnt lgkmcnt(8)\n"
    "v_pk_add_f32 v[192:193], v[192:193], v[120:121]\n"
    "v_pk_add_f32 v[194:195], v[194:195], v[122:123]\n"
    "v_pk_add_f32 v[196:197], v[196:197], v[124:125]\n"
    "v_pk_add_f32 v[198:199], v[198:199], v[126:127]\n"
    "s_waitcnt lgkmcnt(6)\n"
    "v_pk_add_f32 v[200:201], v[200:201], v[128:129]\n"
    "v_pk_add_f32 v[202:203], v[202:203], v[130:131]\n"
    "v_pk_add_f32 v[204:205], v[204:205], v[132:133]\n"
    "v_pk_add_f32 v[206:207], v[206:207], v[134:135]\n"
    "s_waitcnt lgkmcnt(4)\n"
    "v_pk_add_f32 v[200:201], v[200:201], v[136:137]\n"
    "v_pk_add_f32 v[202:203], v[202:203], v[138:139]\n"
    "v_pk_add_f32 v[204:205], v[204:205], v[140:141]\n"
    "v_pk_add_f32 v[206:207], v[206:207], v[142:143]\n"
    "s_waitcnt lgkmcnt(2)\n"
    "v_pk_add_f32 v[200:201], v[200:201], v[144:145]\n"
    "v_pk_add_f32 v[202:203], v[202:203], v[146:147]\n"
    "v_pk_add_f32 v[204:205], v[204:205], v[148:149]\n"
    "v_pk_add_f32 v[206:207], v[206:207], v[150:151]\n"
    "s_waitcnt lgkmcnt(0)\n"
    "v_pk_add_f32 v[200:201], v[200:201], v[152:153]\n"
    "v_pk_add_f32 v[202:203], v[202:203], v[154:155]\n"
    "v_pk_add_f32 v[204:205], v[204:205], v[156:157]\n"
    "v_pk_add_f32 v[206:207], v[206:207], v[158:159]\n"
    "v_cmp_ne_u64 vcc, s[52:53], v[228:229]\n"
    "s_cbranch_vccz Lzok_%=\n"
    "buffer_load_dwordx2 v[228:229], v251, s[16:19], 0 offen sc1\n"
    "s_waitcnt vmcnt(0)\n"
    "v_cmp_ne_u64 vcc, s[52:53], v[228:229]\n"
    "s_cbranch_vccnz Lsp13_%=\n"
    "Lsb13_%=:\n"
    "buffer_load_dwordx4 v[232:235], v252, s[16:19], 0 offen sc1\n"
    "s_mov_b32 s79, 0x20000\n"
    "buffer_load_dwordx4 v[236:239], v252, s[16:19], s79 offen sc1\n"
    "Lzok_%=:\n"
    "s_cmp_lg_u32 s55, 0\n"
    "s_cbranch_scc1 Lepd_%=\n"
    "v_lshlrev_b32 v21, 8, v0\n"
    "v_add_u32 v21, 524288, v21\n"
    "buffer_load_dwordx2 v[228:229], v21, s[16:19], 0 offen sc1\n"
    "s_waitcnt vmcnt(0)\n"
    "v_cmp_ne_u64 vcc, s[52:53], v[228:229]\n"
    "s_cbranch_vccnz Lsp14_%=\n"
    "Lsb14_%=:\n"
    "Lepd_%=:\n"
    "s_waitcnt vmcnt(0)\n"
    "ds_write_b128 v11, v[232:235]\n"
    "ds_write_b128 v224, v[236:239] offset:4096\n"
    "ds_write_b32 v27, v28 offset:96\n"
    "ds_read_b32 v26, v31 offset:96\n"
    "s_waitcnt lgkmcnt(0)\n"
    "v_cmp_ne_u32 vcc, 1, v26\n"
    "s_cbranch_vccnz Lsp15_%=\n"
    "Lsb15_%=:\n"
    "ds_read_u16_d16_hi v64, v12 offset:0\n"
    "ds_read_u16_d16_hi v65, v12 offset:128\n"
    "ds_read_u16_d16_hi v66, v12 offset:256\n"
    "ds_read_u16_d16_hi v67, v12 offset:384\n"
    "ds_read_u16_d16_hi v68, v12 offset:512\n"
    "ds_read_u16_d16_hi v69, v12 offset:640\n"
    "ds_read_u16_d16_hi v70, v12 offset:768\n"
    "ds_read_u16_d16_hi v71, v12 offset:896\n"
    "ds_read_u16_d16_hi v72, v225 offset:0\n"
    "ds_read_u16_d16_hi v73, v225 offset:128\n"
    "ds_read_u16_d16_hi v74, v225 offset:256\n"
    "ds_read_u16_d16_hi v75, v225 offset:384\n"
    "ds_read_u16_d16_hi v76, v225 offset:512\n"
    "ds_read_u16_d16_hi v77, v225 offset:640\n"
    "ds_read_u16_d16_hi v78, v225 offset:768\n"
    "ds_read_u16_d16_hi v79, v225 offset:896\n"
    "s_waitcnt lgkmcnt(8)\n"
    "v_fma_f32 v192, v208, v64, v192\n"
    "v_fma_f32 v193, v209, v65, v193\n"
    "v_fma_f32 v194, v210, v66, v194\n"
    "v_fma_f32 v195, v211, v67, v195\n"
    "v_fma_f32 v196, v212, v68, v196\n"
    "v_fma_f32 v197, v213, v69, v197\n"
    "v_fma_f32 v198, v214, v70, v198\n"
    "v_fma_f32 v199, v215, v71, v199\n"
    "s_waitcnt lgkmcnt(0)\n"
    "v_fma_f32 v200, v216, v72, v200\n"
    "v_fma_f32 v201, v217, v73, v201\n"
    "v_fma_f32 v202, v218, v74, v202\n"
    "v_fma_f32 v203, v219, v75, v203\n"
    "v_fma_f32 v204, v220, v76, v204\n"
    "v_fma_f32 v205, v221, v77, v205\n"
    "v_fma_f32 v206, v222, v78, v206\n"
    "v_fma_f32 v207, v223, v79, v207\n"
    "s_barrier\n"
    "ds_read_b128 v[96:99], v15 offset:32768\n"
    "ds_read_b128 v[100:103], v30 offset:32768\n"
    "ds_read_b128 v[104:107], v15 offset:40960\n"
    "ds_read_b128 v[108:111], v30 offset:40960\n"
    "ds_read_b128 v[112:115], v13 offset:32768\n"
    "ds_read_b128 v[116:119], v14 offset:32768\n"
    "ds_read_b128 v[120:123], v13 offset:40960\n"
    "ds_read_b128 v[124:127], v14 offset:40960\n"
    "ds_read_b128 v[128:131], v29 offset:0\n"
    "ds_read_b128 v[132:135], v10 offset:0\n"
    "ds_read_b128 v[136:139], v29 offset:8192\n"
    "ds_read_b128 v[140:143], v10 offset:8192\n"
    "ds_read_b128 v[144:147], v29 offset:16384\n"
    "ds_read_b128 v[148:151], v10 offset:16384\n"
    "ds_read_b128 v[152:155], v29 offset:24576\n"
    "ds_read_b128 v[156:159], v10 offset:24576\n"
    "s_waitcnt lgkmcnt(14)\n"
    "v_pk_add_f32 v[192:193], v[192:193], v[96:97]\n"
    "v_pk_add_f32 v[194:195], v[194:195], v[98:99]\n"
    "v_pk_add_f32 v[196:197], v[196:197], v[100:101]\n"
    "v_pk_add_f32 v[198:199], v[198:199], v[102:103]\n"
    "s_waitcnt lgkmcnt(12)\n"
    "v_pk_add_f32 v[192:193], v[192:193], v[104:105]\n"
    "v_pk_add_f32 v[194:195], v[194:195], v[106:107]\n"
    "v_pk_add_f32 v[196:197], v[196:197], v[108:109]\n"
    "v_pk_add_f32 v[198:199], v[198:199], v[110:111]\n"
    "s_waitcnt lgkmcnt(10)\n"
    "v_pk_add_f32 v[192:193], v[192:193], v[112:113]\n"
    "v_pk_add_f32 v[194:195], v[194:195], v[114:115]\n"
    "v_pk_add_f32 v[196:197], v[196:197], v[116:117]\n"
    "v_pk_add_f32 v[198:199], v[198:199], v[118:119]\n"
    "s_waitcnt lgkmcnt(8)\n"
    "v_pk_add_f32 v[192:193], v[192:193], v[120:121]\n"
    "v_pk_add_f32 v[194:195], v[194:195], v[122:123]\n"
    "v_pk_add_f32 v[196:197], v[196:197], v[124:125]\n"
    "v_pk_add_f32 v[198:199], v[198:199], v[126:127]\n"
    "v_cvt_pk_bf16_f32 v80, v192, v193\n"
    "v_cvt_pk_bf16_f32 v81, v194, v195\n"
    "v_cvt_pk_bf16_f32 v82, v196, v197\n"
    "v_cvt_pk_bf16_f32 v83, v198, v199\n"
    "buffer_store_dwordx4 v[80:83], v250, s[48:51], 0 offen sc1\n"
    "s_waitcnt lgkmcnt(6)\n"
    "v_pk_add_f32 v[200:201], v[200:201], v[128:129]\n"
    "v_pk_add_f32 v[202:203], v[202:203], v[130:131]\n"
    "v_pk_add_f32 v[204:205], v[204:205], v[132:133]\n"
    "v_pk_add_f32 v[206:207], v[206:207], v[134:135]\n"
    "s_waitcnt lgkmcnt(4)\n"
    "v_pk_add_f32 v[200:201], v[200:201], v[136:137]\n"
    "v_pk_add_f32 v[202:203], v[202:203], v[138:139]\n"
    "v_pk_add_f32 v[204:205], v[204:205], v[140:141]\n"
    "v_pk_add_f32 v[206:207], v[206:207], v[142:143]\n"
    "s_waitcnt lgkmcnt(2)\n"
    "v_pk_add_f32 v[200:201], v[200:201], v[144:145]\n"
    "v_pk_add_f32 v[202:203], v[202:203], v[146:147]\n"
    "v_pk_add_f32 v[204:205], v[204:205], v[148:149]\n"
    "v_pk_add_f32 v[206:207], v[206:207], v[150:151]\n"
    "s_waitcnt lgkmcnt(0)\n"
    "v_pk_add_f32 v[200:201], v[200:201], v[152:153]\n"
    "v_pk_add_f32 v[202:203], v[202:203], v[154:155]\n"
    "v_pk_add_f32 v[204:205], v[204:205], v[156:157]\n"
    "v_pk_add_f32 v[206:207], v[206:207], v[158:159]\n"
    "v_cvt_pk_bf16_f32 v84, v200, v201\n"
    "v_cvt_pk_bf16_f32 v85, v202, v203\n"
    "v_cvt_pk_bf16_f32 v86, v204, v205\n"
    "v_cvt_pk_bf16_f32 v87, v206, v207\n"
    "buffer_store_dwordx4 v[84:87], v250, s[48:51], s76 offen sc1\n"
    "s_or_b32 s77, s55, s56\n"
    "s_cbranch_scc1 Ldone_%=\n"
    "buffer_store_dwordx2 v[18:19], v9, s[16:19], s77 offen offset:0 sc1\n"
    "Ldone_%=:\n"
    "s_endpgm\n"
    "Lsg_%=:\n"
    "v_and_b32 v29, 0xff0, v3\n"
    "v_add_u32 v128, -8, v29\n"
    "v_cmp_eq_u32 vcc, 0, v29\n"
    "s_lshl_b32 s75, s55, 13\n"
    "s_add_u32 s76, s75, 4096\n"
    "v_cndmask_b32 v128, v128, v10, vcc\n"
    "s_lshl_b32 s77, s55, 8\n"
    "s_lshl_b32 s78, s63, 19\n"
    "s_add_u32 s77, s77, s78\n"
    "s_add_u32 s77, s77, 1048576\n"
    "v_lshrrev_b32 v23, 3, v1\n"
    "v_lshlrev_b32 v23, 16, v23\n"
    "v_lshl_add_u32 v23, v16, 4, v23\n"
    "v_add_u32 v129, s77, v23\n"
    "s_lshl_b32 s78, s63, 10\n"
    "s_add_u32 s78, s78, 132032\n"
    "v_add_u32 v130, s78, v2\n"
    "s_lshr_b32 s77, s55, 3\n"
    "s_lshl2_add_u32 s77, s77, s63\n"
    "s_lshl_b32 s77, s77, 10\n"
    "s_add_u32 s77, s77, 786432\n"
    "v_lshl_add_u32 v131, v16, 7, s77\n"
    "buffer_load_dwordx4 v[148:151], v29, s[12:15], s75 offen\n"
    "buffer_load_dwordx2 v[156:157], v128, s[12:15], s75 offen\n"
    "buffer_load_dwordx4 v[152:155], v29, s[12:15], s76 offen\n"
    "buffer_load_dwordx2 v[158:159], v128, s[12:15], s76 offen\n"
    "v_and_b32 v21, 3, v0\n"
    "v_mul_u32_u24 v21, 4160, v21\n"
    "v_lshrrev_b32 v22, 2, v4\n"
    "v_sub_u32 v22, v22, v5\n"
    "s_mul_i32 s77, s64, 16640\n"
    "s_add_u32 s77, s77, 98352\n"
    "v_lshl_add_u32 v21, v22, 4, v21\n"
    "v_add_u32 v240, s77, v21\n"
    "v_and_b32 v23, 1, v0\n"
    "v_xor_b32 v21, 1, v23\n"
    "v_mul_u32_u24 v21, 4640, v21\n"
    "v_lshrrev_b32 v22, 1, v4\n"
    "v_lshlrev_b32 v22, 6, v22\n"
    "v_lshl_add_u32 v22, v5, 4, v22\n"
    "s_mul_i32 s77, s64, 9280\n"
    "s_add_u32 s77, s77, 131584\n"
    "v_add3_u32 v241, v21, v22, s77\n"
    "v_lshl_add_u32 v242, v23, 3, v22\n"
    "v_add_u32 v243, s77, v242\n"
    "s_cmp_eq_u32 s65, 0\n"
    "s_movk_i32 s77, 0x1256\n"
    "s_cselect_b32 s77, 0x0347, s77\n"
    "v_lshlrev_b32 v21, 2, v5\n"
    "v_bfe_u32 v21, s77, v21, 3\n"
    "v_lshrrev_b32 v22, 2, v4\n"
    "v_lshl_add_u32 v21, v21, 2, v22\n"
    "v_and_b32 v22, 3, v0\n"
    "v_lshl_add_u32 v21, v21, 3, v22\n"
    "v_lshlrev_b32 v242, 4, v21\n"
    "s_mov_b32 s71, s67\n"
    "s_add_u32 s72, s67, 16384\n"
    "s_mul_i32 s77, s63, 14\n"
    "s_mul_i32 s78, s61, 56\n"
    "s_add_u32 s77, s77, s78\n"
    "s_lshl_b32 s70, s77, 12\n"
    "s_add_u32 s70, s70, 5242880\n"
    "s_lshl_b32 s77, s77, 7\n"
    "s_add_u32 s77, s77, 263168\n"
    "v_min_u32 v17, 2, v1\n"
    "v_lshl_add_u32 v17, v17, 8, s77\n"
    "v_lshrrev_b32 v21, 3, v0\n"
    "v_lshlrev_b32 v22, 4, v16\n"
    "s_lshl_b32 s77, s61, 16\n"
    "s_lshl_b32 s78, s62, 7\n"
    "s_add_u32 s77, s77, s78\n"
    "v_lshl_add_u32 v23, v21, 10, v22\n"
    "v_add_u32 v250, s77, v23\n"
    "v_add_u32 v254, s78, v22\n"
    "s_lshl_b32 s77, s62, 18\n"
    "s_lshl_b32 s78, s61, 7\n"
    "s_add_u32 s77, s77, s78\n"
    "s_add_u32 s77, s77, 3145728\n"
    "v_lshl_add_u32 v23, v21, 12, v22\n"
    "v_add_u32 v252, s77, v23\n"
    "s_lshl_b32 s77, s61, 10\n"
    "s_add_u32 s77, s77, 131072\n"
    "v_lshl_add_u32 v253, v21, 4, s77\n"
    "s_lshr_b32 s77, s61, 2\n"
    "s_lshr_b32 s77, 0x66, s77\n"
    "s_and_b32 s77, s77, 1\n"
    "s_lshl_b32 s77, s77, 16\n"
    "s_lshl3_add_u32 s78, s62, s63\n"
    "s_lshl_b32 s78, s78, 10\n"
    "s_add_u32 s77, s77, s78\n"
    "s_add_u32 s77, s77, 524288\n"
    "v_lshl_add_u32 v251, v16, 7, s77\n"
    "v_and_b32 v21, 8, v0\n"
    "v_lshl_add_u32 v251, v21, 9, v251\n"
    "v_xor_b32 v21, v16, v5\n"
    "v_lshlrev_b32 v21, 4, v21\n"
    "v_lshl_add_u32 v21, v4, 8, v21\n"
    "v_add_u32 v6, s71, v21\n"
    "v_add_u32 v24, s72, v21\n"
    "v_xor_b32 v20, 64, v6\n"
    "v_xor_b32 v25, 64, v24\n"
    "s_lshr_b32 s77, s57, 1\n"
    "s_mul_i32 s77, s77, 1\n"
    "s_add_u32 s28, s77, 0\n"
    "s_and_b32 s28, s28, 3\n"
    "s_lshl_b32 s28, s28, 13\n"
    "s_add_u32 s29, s77, 1\n"
    "s_and_b32 s29, s29, 3\n"
    "s_lshl_b32 s29, s29, 13\n"
    "s_add_u32 s30, s77, 2\n"
    "s_and_b32 s30, s30, 3\n"
    "s_lshl_b32 s30, s30, 13\n"
    "s_add_u32 s31, s77, 3\n"
    "s_and_b32 s31, s31, 3\n"
    "s_lshl_b32 s31, s31, 13\n"
    "s_lshl_b32 s77, s77, 1\n"
    "s_lshr_b32 s77, 0x924924, s77\n"
    "s_bfe_u32 s32, s77, 0x20000\n"
    "s_add_u32 s32, s32, 4\n"
    "s_lshl_b32 s32, s32, 13\n"
    "s_bfe_u32 s33, s77, 0x20002\n"
    "s_add_u32 s33, s33, 4\n"
    "s_lshl_b32 s33, s33, 13\n"
    "s_bfe_u32 s34, s77, 0x20004\n"
    "s_add_u32 s34, s34, 4\n"
    "s_lshl_b32 s34, s34, 13\n"
    "s_waitcnt lgkmcnt(0)\n"
    "s_barrier\n"
    "ds_read_b128 v[32:35], v224\n"
    "ds_read_b128 v[36:39], v225\n"
    "ds_read_b128 v[40:43], v226\n"
    "ds_read_b128 v[44:47], v227\n"
    "ds_read_b128 v[48:51], v228\n"
    "ds_read_b128 v[52:55], v229\n"
    "ds_read_b128 v[56:59], v230\n"
    "ds_read_b128 v[60:63], v231\n"
    "ds_read_b64 v[88:89], v232 offset:0\n"
    "ds_read_b64 v[90:91], v232 offset:128\n"
    "ds_read_b64 v[92:93], v232 offset:256\n"
    "ds_read_b64 v[94:95], v232 offset:384\n"
    "s_waitcnt lgkmcnt(11)\n"
    "v_mfma_f32_16x16x32_bf16 v[64:67], v[80:83], v[32:35], 0\n"
    "s_waitcnt lgkmcnt(10)\n"
    "v_mfma_f32_16x16x32_bf16 v[64:67], v[84:87], v[36:39], v[64:67]\n"
    "s_waitcnt lgkmcnt(9)\n"
    "v_mfma_f32_16x16x32_bf16 v[68:71], v[80:83], v[40:43], 0\n"
    "s_waitcnt lgkmcnt(8)\n"
    "v_mfma_f32_16x16x32_bf16 v[68:71], v[84:87], v[44:47], v[68:71]\n"
    "s_waitcnt lgkmcnt(7)\n"
    "v_mfma_f32_16x16x32_bf16 v[72:75], v[80:83], v[48:51], 0\n"
    "s_waitcnt lgkmcnt(6)\n"
    "v_mfma_f32_16x16x32_bf16 v[72:75], v[84:87], v[52:55], v[72:75]\n"
    "s_waitcnt lgkmcnt(5)\n"
    "v_mfma_f32_16x16x32_bf16 v[76:79], v[80:83], v[56:59], 0\n"
    "s_waitcnt lgkmcnt(4)\n"
    "v_mfma_f32_16x16x32_bf16 v[76:79], v[84:87], v[60:63], v[76:79]\n"
    "s_waitcnt lgkmcnt(0)\n"
    "v_lshlrev_b32 v32, 16, v88\n"
    "v_and_b32 v33, 0xffff0000, v88\n"
    "v_lshlrev_b32 v34, 16, v89\n"
    "v_and_b32 v35, 0xffff0000, v89\n"
    "v_lshlrev_b32 v36, 16, v90\n"
    "v_and_b32 v37, 0xffff0000, v90\n"
    "v_lshlrev_b32 v38, 16, v91\n"
    "v_and_b32 v39, 0xffff0000, v91\n"
    "v_lshlrev_b32 v40, 16, v92\n"
    "v_and_b32 v41, 0xffff0000, v92\n"
    "v_lshlrev_b32 v42, 16, v93\n"
    "v_and_b32 v43, 0xffff0000, v93\n"
    "v_lshlrev_b32 v44, 16, v94\n"
    "v_and_b32 v45, 0xffff0000, v94\n"
    "v_lshlrev_b32 v46, 16, v95\n"
    "v_and_b32 v47, 0xffff0000, v95\n"
    "v_pk_mul_f32 v[32:33], v[32:33], v[64:65]\n"
    "v_pk_mul_f32 v[34:35], v[34:35], v[66:67]\n"
    "v_pk_mul_f32 v[36:37], v[36:37], v[68:69]\n"
    "v_pk_mul_f32 v[38:39], v[38:39], v[70:71]\n"
    "v_pk_mul_f32 v[40:41], v[40:41], v[72:73]\n"
    "v_pk_mul_f32 v[42:43], v[42:43], v[74:75]\n"
    "v_pk_mul_f32 v[44:45], v[44:45], v[76:77]\n"
    "v_pk_mul_f32 v[46:47], v[46:47], v[78:79]\n"
    "v_cvt_pk_bf16_f32 v48, v32, v33\n"
    "v_cvt_pk_bf16_f32 v49, v36, v37\n"
    "v_cvt_pk_bf16_f32 v50, v40, v41\n"
    "v_cvt_pk_bf16_f32 v51, v44, v45\n"
    "buffer_store_dwordx4 v[48:51], v233, s[16:19], s36 offen offset:0 sc1\n"
    "v_cvt_pk_bf16_f32 v52, v34, v35\n"
    "v_cvt_pk_bf16_f32 v53, v38, v39\n"
    "v_cvt_pk_bf16_f32 v54, v42, v43\n"
    "v_cvt_pk_bf16_f32 v55, v46, v47\n"
    "buffer_store_dwordx4 v[52:55], v233, s[16:19], s36 offen offset:256 sc1\n"
    "s_barrier\n"
    "v_mov_b32 v92, 0\n"
    "v_mov_b32 v93, 0\n"
    "v_mov_b32 v94, 0\n"
    "v_mov_b32 v95, 0\n"
    "s_mov_b64 exec, 0x1fffffff\n"
    "ds_write_b128 v239, v[92:95]\n"
    "s_mov_b64 exec, -1\n"
    "s_waitcnt vmcnt(2)\n"
    "s_mov_b32 s75, 0xffff\n"
    "v_add_u32 v29, 98352, v29\n"
    "v_alignbit_b32 v64, v151, v151, 16\n"
    "v_alignbit_b32 v65, v150, v150, 16\n"
    "v_alignbit_b32 v66, v149, v149, 16\n"
    "v_alignbit_b32 v67, v148, v148, 16\n"
    "ds_write_b128 v29, v[64:67] offset:12480\n"
    "v_alignbit_b32 v68, v150, v150, 16\n"
    "v_alignbit_b32 v69, v149, v149, 16\n"
    "v_alignbit_b32 v70, v148, v148, 16\n"
    "v_alignbit_b32 v71, v157, v157, 16\n"
    "ds_write_b128 v29, v[68:71] offset:4160\n"
    "v_bfi_b32 v72, s75, v151, v150\n"
    "v_bfi_b32 v73, s75, v150, v149\n"
    "v_bfi_b32 v74, s75, v149, v148\n"
    "v_bfi_b32 v75, s75, v148, v157\n"
    "ds_write_b128 v29, v[72:75] offset:8320\n"
    "v_bfi_b32 v76, s75, v150, v149\n"
    "v_bfi_b32 v77, s75, v149, v148\n"
    "v_bfi_b32 v78, s75, v148, v157\n"
    "v_bfi_b32 v79, s75, v157, v156\n"
    "ds_write_b128 v29, v[76:79] offset:0\n"
    "v_alignbit_b32 v80, v155, v155, 16\n"
    "v_alignbit_b32 v81, v154, v154, 16\n"
    "v_alignbit_b32 v82, v153, v153, 16\n"
    "v_alignbit_b32 v83, v152, v152, 16\n"
    "ds_write_b128 v29, v[80:83] offset:29120\n"
    "v_alignbit_b32 v84, v154, v154, 16\n"
    "v_alignbit_b32 v85, v153, v153, 16\n"
    "v_alignbit_b32 v86, v152, v152, 16\n"
    "v_alignbit_b32 v87, v159, v159, 16\n"
    "ds_write_b128 v29, v[84:87] offset:20800\n"
    "v_bfi_b32 v88, s75, v155, v154\n"
    "v_bfi_b32 v89, s75, v154, v153\n"
    "v_bfi_b32 v90, s75, v153, v152\n"
    "v_bfi_b32 v91, s75, v152, v159\n"
    "ds_write_b128 v29, v[88:91] offset:24960\n"
    "v_bfi_b32 v92, s75, v154, v153\n"
    "v_bfi_b32 v93, s75, v153, v152\n"
    "v_bfi_b32 v94, s75, v152, v159\n"
    "v_bfi_b32 v95, s75, v159, v158\n"
    "ds_write_b128 v29, v[92:95] offset:16640\n"
    "s_waitcnt vmcnt(0)\n"
    "buffer_store_dwordx2 v[18:19], v9, s[16:19], s37 offen offset:0 sc1\n"
    "s_waitcnt lgkmcnt(0)\n"
    "s_barrier\n"
    "s_sleep 50\n"
    "buffer_load_dwordx2 v[168:169], v131, s[16:19], 0 offen sc1\n"
    "s_waitcnt vmcnt(0)\n"
    "v_cmp_ne_u64 vcc, s[52:53], v[168:169]\n"
    "s_cbranch_vccnz Lsp16_%=\n"
    "Lsb16_%=:\n"
    "buffer_load_dwordx4 v[172:175], v129, s[16:19], 0 offen sc1\n"
    "buffer_load_dwordx4 v[176:179], v129, s[16:19], 0 offen offset:128 sc1\n"
    "s_mov_b32 s77, 0x05040100\n"
    "s_mov_b32 s78, 0x07060302\n"
    "s_waitcnt vmcnt(0)\n"
    "v_perm_b32 v184, v173, v172, s77\n"
    "v_perm_b32 v185, v175, v174, s77\n"
    "v_perm_b32 v186, v177, v176, s77\n"
    "v_perm_b32 v187, v179, v178, s77\n"
    "ds_write_b128 v130, v[184:187] offset:0\n"
    "ds_write_b64 v130, v[184:185] offset:4648\n"
    "ds_write_b64 v130, v[186:187] offset:4656\n"
    "v_perm_b32 v188, v173, v172, s78\n"
    "v_perm_b32 v189, v175, v174, s78\n"
    "v_perm_b32 v190, v177, v176, s78\n"
    "v_perm_b32 v191, v179, v178, s78\n"
    "ds_write_b128 v130, v[188:191] offset:9280\n"
    "ds_write_b64 v130, v[188:189] offset:13928\n"
    "ds_write_b64 v130, v[190:191] offset:13936\n"
    "ds_write_b32 v27, v28 offset:16\n"
    "ds_read_b32 v26, v31 offset:16\n"
    "s_waitcnt vmcnt(0) lgkmcnt(0)\n"
    "s_branch Lconv_%=\n"
    "Lsl_%=:\n"
    "v_and_b32 v21, 0xff, v0\n"
    "v_lshlrev_b32 v21, 7, v21\n"
    "s_and_b32 s77, s58, 3\n"
    "s_lshl2_add_u32 s77, s57, s77\n"
    "s_lshl_b32 s77, s77, 16\n"
    "s_add_u32 s78, s77, 0x8000\n"
    "v_mov_b32 v129, v21\n"
    "s_mov_b32 s75, s77\n"
    "s_mov_b32 s76, s78\n"
    "s_lshr_b32 s77, s55, 3\n"
    "s_lshl_b32 s77, s77, 16\n"
    "s_and_b32 s78, s58, 7\n"
    "s_lshl_b32 s78, s78, 7\n"
    "s_add_u32 s77, s77, s78\n"
    "v_lshrrev_b32 v23, 3, v1\n"
    "v_lshrrev_b32 v24, 5, v1\n"
    "v_xor_b32 v25, v16, v24\n"
    "v_lshl_add_u32 v21, v23, 10, s77\n"
    "v_lshl_add_u32 v21, v25, 4, v21\n"
    "v_add_u32 v142, -8192, v21\n"
    "s_cmp_lt_u32 s55, 8\n"
    "s_cselect_b64 vcc, -1, 0\n"
    "s_lshl_b32 s77, s63, 14\n"
    "s_lshl_b32 s78, s64, 6\n"
    "v_cndmask_b32 v142, v142, v10, vcc\n"
    "v_xor_b32 v21, s78, v21\n"
    "v_add_u32 v141, s77, v21\n"
    "v_xor_b32 v139, 32, v141\n"
    "v_xor_b32 v140, 64, v141\n"
    "s_lshl_b32 s69, s63, 11\n"
    "s_add_u32 s69, s69, 131584\n"
    "s_lshl_b32 s77, s63, 3\n"
    "s_lshl_b32 s98, 0xff, s77\n"
    "s_mov_b32 m0, 131584\n"
    "s_mov_b32 exec_lo, 0\n"
    "s_mov_b32 exec_hi, s98\n"
    "buffer_load_dwordx4 v142, s[28:31], 0 offen lds\n"
    "s_mov_b64 exec, -1\n"
    "s_add_u32 m0, s69, 1024\n"
    "s_mov_b32 s79, 0\n"
    "buffer_load_dwordx4 v139, s[28:31], s79 offen offset:0 lds\n"
    "s_add_u32 m0, s69, 2048\n"
    "s_mov_b32 s79, 8192\n"
    "buffer_load_dwordx4 v140, s[28:31], s79 offen offset:0 lds\n"
    "s_add_u32 m0, s69, 9216\n"
    "s_mov_b32 s79, 0\n"
    "buffer_load_dwordx4 v141, s[32:35], s79 offen offset:0 lds\n"
    "s_add_u32 m0, s69, 10240\n"
    "s_mov_b32 s79, 8192\n"
    "buffer_load_dwordx4 v139, s[32:35], s79 offen offset:0 lds\n"
    "s_setprio 0\n"
    "v_bfe_u32 v21, v0, 2, 3\n"
    "v_and_b32 v22, 3, v0\n"
    "v_lshrrev_b32 v23, 3, v1\n"
    "v_and_b32 v24, 4, v23\n"
    "v_and_b32 v25, 2, v23\n"
    "v_xor_b32 v25, v25, v22\n"
    "v_or_b32 v25, v25, v24\n"
    "s_mul_i32 s77, s63, 458752\n"
    "s_lshl_b32 s78, s62, 7\n"
    "s_add_u32 s77, s77, s78\n"
    "v_lshlrev_b32 v25, 4, v25\n"
    "v_lshl_add_u32 v25, v21, 10, v25\n"
    "v_add_u32 v6, s77, v25\n"
    "s_lshr_b32 s77, s57, 1\n"
    "s_mul_i32 s77, s77, 1\n"
    "s_add_u32 s28, s77, 0\n"
    "s_and_b32 s28, s28, 3\n"
    "s_lshl_b32 s28, s28, 13\n"
    "s_add_u32 s29, s77, 1\n"
    "s_and_b32 s29, s29, 3\n"
    "s_lshl_b32 s29, s29, 13\n"
    "s_add_u32 s30, s77, 2\n"
    "s_and_b32 s30, s30, 3\n"
    "s_lshl_b32 s30, s30, 13\n"
    "s_add_u32 s31, s77, 3\n"
    "s_and_b32 s31, s31, 3\n"
    "s_lshl_b32 s31, s31, 13\n"
    "s_lshl_b32 s77, s77, 1\n"
    "s_lshr_b32 s77, 0x924924, s77\n"
    "s_bfe_u32 s32, s77, 0x20000\n"
    "s_add_u32 s32, s32, 4\n"
    "s_lshl_b32 s32, s32, 13\n"
    "s_bfe_u32 s33, s77, 0x20002\n"
    "s_add_u32 s33, s33, 4\n"
    "s_lshl_b32 s33, s33, 13\n"
    "s_bfe_u32 s34, s77, 0x20004\n"
    "s_add_u32 s34, s34, 4\n"
    "s_lshl_b32 s34, s34, 13\n"
    "s_mul_i32 s77, s63, 14\n"
    "s_mul_i32 s78, s61, 56\n"
    "s_add_u32 s77, s77, s78\n"
    "s_lshl_b32 s73, s77, 12\n"
    "s_add_u32 s73, s73, 5242880\n"
    "s_lshl_b32 s77, s77, 7\n"
    "s_add_u32 s77, s77, 262144\n"
    "v_min_u32 v17, 6, v1\n"
    "v_lshl_add_u32 v17, v17, 8, s77\n"
    "v_bfrev_b32 v15, 1\n"
    "s_waitcnt lgkmcnt(0)\n"
    "s_add_u32 s52, s52, 0x7f4a7c15\n"
    "s_addc_u32 s53, s53, 0x9e3779b9\n"
    "v_mov_b32 v18, s52\n"
    "v_mov_b32 v19, s53\n"
    "s_add_u32 m0, s67, 32768\n"
    "s_lshl3_add_u32 s79, s28, 0\n"
    "buffer_load_dwordx4 v6, s[24:27], s79 offen offset:0 lds\n"
    "s_add_u32 m0, s67, 33792\n"
    "s_lshl3_add_u32 s79, s28, 8192\n"
    "buffer_load_dwordx4 v6, s[24:27], s79 offen offset:0 lds\n"
    "s_add_u32 m0, s67, 34816\n"
    "s_lshl3_add_u32 s79, s28, 16384\n"
    "buffer_load_dwordx4 v6, s[24:27], s79 offen offset:0 lds\n"
    "s_add_u32 m0, s67, 35840\n"
    "s_lshl3_add_u32 s79, s28, 24576\n"
    "buffer_load_dwordx4 v6, s[24:27], s79 offen offset:0 lds\n"
    "s_waitcnt vmcnt(4)\n"
    "s_barrier\n"
    "s_add_u32 m0, s67, 36864\n"
    "s_lshl3_add_u32 s79, s28, 32768\n"
    "buffer_load_dwordx4 v6, s[24:27], s79 offen offset:0 lds\n"
    "s_add_u32 m0, s67, 37888\n"
    "s_lshl3_add_u32 s79, s28, 40960\n"
    "buffer_load_dwordx4 v6, s[24:27], s79 offen offset:0 lds\n"
    "s_add_u32 m0, s67, 38912\n"
    "s_lshl3_add_u32 s79, s28, 49152\n"
    "buffer_load_dwordx4 v6, s[24:27], s79 offen offset:0 lds\n"
    "s_add_u32 m0, s67, 39936\n"
    "s_lshl3_add_u32 s79, s28, 57344\n"
    "buffer_load_dwordx4 v6, s[24:27], s79 offen offset:0 lds\n"
    "s_barrier\n"
    "s_add_u32 m0, s67, 0\n"
    "s_lshl3_add_u32 s79, s29, 0\n"
    "buffer_load_dwordx4 v6, s[24:27], s79 offen offset:0 lds\n"
    "s_add_u32 m0, s67, 1024\n"
    "s_lshl3_add_u32 s79, s29, 8192\n"
    "buffer_load_dwordx4 v6, s[24:27], s79 offen offset:0 lds\n"
    "s_add_u32 m0, s67, 2048\n"
    "s_lshl3_add_u32 s79, s29, 16384\n"
    "buffer_load_dwordx4 v6, s[24:27], s79 offen offset:0 lds\n"
    "s_add_u32 m0, s67, 3072\n"
    "s_lshl3_add_u32 s79, s29, 24576\n"
    "buffer_load_dwordx4 v6, s[24:27], s79 offen offset:0 lds\n"
    "s_add_u32 m0, s67, 4096\n"
    "s_lshl3_add_u32 s79, s29, 32768\n"
    "buffer_load_dwordx4 v6, s[24:27], s79 offen offset:0 lds\n"
    "s_add_u32 m0, s67, 5120\n"
    "s_lshl3_add_u32 s79, s29, 40960\n"
    "buffer_load_dwordx4 v6, s[24:27], s79 offen offset:0 lds\n"
    "s_add_u32 m0, s67, 6144\n"
    "s_lshl3_add_u32 s79, s29, 49152\n"
    "buffer_load_dwordx4 v6, s[24:27], s79 offen offset:0 lds\n"
    "s_add_u32 m0, s67, 7168\n"
    "s_lshl3_add_u32 s79, s29, 57344\n"
    "buffer_load_dwordx4 v6, s[24:27], s79 offen offset:0 lds\n"
    "s_barrier\n"
    "buffer_load_dword v100, v129, s[12:15], s75 offen\n"
    "buffer_load_dword v101, v129, s[12:15], s76 offen\n"
    "s_add_u32 m0, s67, 16384\n"
    "s_lshl3_add_u32 s79, s30, 0\n"
    "buffer_load_dwordx4 v6, s[24:27], s79 offen offset:0 lds\n"
    "s_waitcnt vmcnt(11)\n"
    "ds_read_b64_tr_b16 v[192:193], v7 offset:32768\n"
    "ds_read_b64_tr_b16 v[194:195], v7 offset:34816\n"
    "ds_read_b64_tr_b16 v[196:197], v8 offset:32768\n"
    "ds_read_b64_tr_b16 v[198:199], v8 offset:34816\n"
    "ds_read_b64_tr_b16 v[200:201], v7 offset:33280\n"
    "ds_read_b64_tr_b16 v[202:203], v7 offset:35328\n"
    "ds_read_b64_tr_b16 v[204:205], v8 offset:33280\n"
    "ds_read_b64_tr_b16 v[206:207], v8 offset:35328\n"
    "ds_read_b64_tr_b16 v[208:209], v7 offset:36864\n"
    "ds_read_b64_tr_b16 v[210:211], v7 offset:38912\n"
    "ds_read_b64_tr_b16 v[212:213], v8 offset:36864\n"
    "ds_read_b64_tr_b16 v[214:215], v8 offset:38912\n"
    "ds_read_b64_tr_b16 v[216:217], v7 offset:37376\n"
    "ds_read_b64_tr_b16 v[218:219], v7 offset:39424\n"
    "ds_read_b64_tr_b16 v[220:221], v8 offset:37376\n"
    "ds_read_b64_tr_b16 v[222:223], v8 offset:39424\n"
    "s_add_u32 m0, s67, 17408\n"
    "s_lshl3_add_u32 s79, s30, 8192\n"
    "buffer_load_dwordx4 v6, s[24:27], s79 offen offset:0 lds\n"
    "s_add_u32 m0, s67, 18432\n"
    "s_lshl3_add_u32 s79, s30, 16384\n"
    "buffer_load_dwordx4 v6, s[24:27], s79 offen offset:0 lds\n"
    "s_add_u32 m0, s67, 19456\n"
    "s_lshl3_add_u32 s79, s30, 24576\n"
    "buffer_load_dwordx4 v6, s[24:27], s79 offen offset:0 lds\n"
    "s_add_u32 m0, s67, 20480\n"
    "s_lshl3_add_u32 s79, s30, 32768\n"
    "buffer_load_dwordx4 v6, s[24:27], s79 offen offset:0 lds\n"
    "s_add_u32 m0, s67, 21504\n"
    "s_lshl3_add_u32 s79, s30, 40960\n"
    "buffer_load_dwordx4 v6, s[24:27], s79 offen offset:0 lds\n"
    "s_add_u32 m0, s67, 22528\n"
    "s_lshl3_add_u32 s79, s30, 49152\n"
    "buffer_load_dwordx4 v6, s[24:27], s79 offen offset:0 lds\n"
    "s_add_u32 m0, s67, 23552\n"
    "s_lshl3_add_u32 s79, s30, 57344\n"
    "buffer_load_dwordx4 v6, s[24:27], s79 offen offset:0 lds\n"
    "s_waitcnt lgkmcnt(0)\n"
    "s_add_u32 m0, s67, 32768\n"
    "s_lshl3_add_u32 s79, s31, 0\n"
    "buffer_load_dwordx4 v6, s[24:27], s79 offen offset:0 lds\n"
    "s_add_u32 m0, s67, 33792\n"
    "s_lshl3_add_u32 s79, s31, 8192\n"
    "buffer_load_dwordx4 v6, s[24:27], s79 offen offset:0 lds\n"
    "s_waitcnt vmcnt(12)\n"
    "ds_read_b64_tr_b16 v[224:225], v7 offset:0\n"
    "ds_read_b64_tr_b16 v[226:227], v7 offset:2048\n"
    "ds_read_b64_tr_b16 v[228:229], v8 offset:0\n"
    "ds_read_b64_tr_b16 v[230:231], v8 offset:2048\n"
    "ds_read_b64_tr_b16 v[232:233], v7 offset:512\n"
    "ds_read_b64_tr_b16 v[234:235], v7 offset:2560\n"
    "ds_read_b64_tr_b16 v[236:237], v8 offset:512\n"
    "ds_read_b64_tr_b16 v[238:239], v8 offset:2560\n"
    "ds_read_b64_tr_b16 v[240:241], v7 offset:4096\n"
    "ds_read_b64_tr_b16 v[242:243], v7 offset:6144\n"
    "ds_read_b64_tr_b16 v[244:245], v8 offset:4096\n"
    "ds_read_b64_tr_b16 v[246:247], v8 offset:6144\n"
    "ds_read_b64_tr_b16 v[248:249], v7 offset:4608\n"
    "ds_read_b64_tr_b16 v[250:251], v7 offset:6656\n"
    "ds_read_b64_tr_b16 v[252:253], v8 offset:4608\n"
    "ds_read_b64_tr_b16 v[254:255], v8 offset:6656\n"
    "s_add_u32 m0, s67, 34816\n"
    "s_lshl3_add_u32 s79, s31, 16384\n"
    "buffer_load_dwordx4 v6, s[24:27], s79 offen offset:0 lds\n"
    "s_add_u32 m0, s67, 35840\n"
    "s_lshl3_add_u32 s79, s31, 24576\n"
    "buffer_load_dwordx4 v6, s[24:27], s79 offen offset:0 lds\n"
    "s_add_u32 m0, s67, 36864\n"
    "s_lshl3_add_u32 s79, s31, 32768\n"
    "buffer_load_dwordx4 v6, s[24:27], s79 offen offset:0 lds\n"
    "s_add_u32 m0, s67, 37888\n"
    "s_lshl3_add_u32 s79, s31, 40960\n"
    "buffer_load_dwordx4 v6, s[24:27], s79 offen offset:0 lds\n"
    "s_add_u32 m0, s67, 38912\n"
    "s_lshl3_add_u32 s79, s31, 49152\n"
    "buffer_load_dwordx4 v6, s[24:27], s79 offen offset:0 lds\n"
    "s_add_u32 m0, s67, 39936\n"
    "s_lshl3_add_u32 s79, s31, 57344\n"
    "buffer_load_dwordx4 v6, s[24:27], s79 offen offset:0 lds\n"
    "s_waitcnt lgkmcnt(0)\n"
    "s_branch Lb4_%=\n"
    "Lsp0_%=:\n"
    "s_mov_b32 s3, 262144\n"
    "Lsq0_%=:\n"
    "s_sleep 6\n"
    "buffer_load_dwordx4 v[164:167], v132, s[16:19], 0 offen sc1\n"
    "s_waitcnt vmcnt(0)\n"
    "v_cmp_ne_u32 vcc, s52, v165\n"
    "v_cmp_ne_u32 s[100:101], s53, v167\n"
    "s_or_b64 vcc, vcc, s[100:101]\n"
    "s_cbranch_scc0 Lsb0_%=\n"
    "s_sub_u32 s3, s3, 1\n"
    "s_cbranch_scc0 Lsq0_%=\n"
    "s_branch Lsb0_%=\n"
    "Lsp1_%=:\n"
    "s_mov_b32 s3, 262144\n"
    "Lsq1_%=:\n"
    "s_sleep 6\n"
    "buffer_load_dwordx4 v[160:163], v134, s[16:19], 0 offen sc1\n"
    "s_waitcnt vmcnt(0)\n"
    "v_cmp_ne_u32 vcc, s52, v161\n"
    "v_cmp_ne_u32 s[100:101], s53, v163\n"
    "s_or_b64 vcc, vcc, s[100:101]\n"
    "s_cbranch_scc0 Lsb1_%=\n"
    "s_sub_u32 s3, s3, 1\n"
    "s_cbranch_scc0 Lsq1_%=\n"
    "s_branch Lsb1_%=\n"
    "Lsp2_%=:\n"
    "s_mov_b32 s3, 262144\n"
    "Lsq2_%=:\n"
    "s_sleep 6\n"
    "buffer_load_dwordx2 v[168:169], v131, s[16:19], 0 offen sc1\n"
    "s_waitcnt vmcnt(0)\n"
    "v_cmp_ne_u64 vcc, s[52:53], v[168:169]\n"
    "s_cbranch_vccz Lsb2_%=\n"
    "s_sub_u32 s3, s3, 1\n"
    "s_cbranch_scc0 Lsq2_%=\n"
    "s_branch Lsb2_%=\n"
    "Lsp3_%=:\n"
    "s_mov_b32 s3, 16777216\n"
    "Lsq3_%=:\n"
    "s_sleep 1\n"
    "ds_read_b32 v26, v31 offset:0\n"
    "s_waitcnt lgkmcnt(0)\n"
    "v_cmp_ne_u32 vcc, 1, v26\n"
    "s_cbranch_vccz Lsb3_%=\n"
    "s_sub_u32 s3, s3, 1\n"
    "s_cbranch_scc0 Lsq3_%=\n"
    "s_branch Lsb3_%=\n"
    "Lsp4_%=:\n"
    "s_mov_b32 s3, 16777216\n"
    "Lsq4_%=:\n"
    "s_sleep 2\n"
    "ds_read_b32 v26, v31 offset:64\n"
    "s_waitcnt lgkmcnt(0)\n"
    "v_cmp_ne_u32 vcc, 1, v26\n"
    "s_cbranch_vccz Lsb4_%=\n"
    "s_sub_u32 s3, s3, 1\n"
    "s_cbranch_scc0 Lsq4_%=\n"
    "s_branch Lsb4_%=\n"
    "Lsp5_%=:\n"
    "s_cmp_lt_u32 s58, 28\n"
    "s_cbranch_scc1 Lsw2_%=\n"
    "s_mov_b32 s3, 0x400000\n"
    "Lsw4_%=:\n"
    "s_sleep 4\n"
    "ds_read_b32 v26, v31 offset:48\n"
    "s_waitcnt lgkmcnt(0)\n"
    "v_cmp_ne_u32 vcc, 1, v26\n"
    "s_cbranch_vccz Lsw5_%=\n"
    "s_sub_u32 s3, s3, 1\n"
    "s_cbranch_scc0 Lsw4_%=\n"
    "Lsw5_%=:\n"
    "s_sleep 2\n"
    "Lsw2_%=:\n"
    "s_mov_b32 s3, 262144\n"
    "Lsq5_%=:\n"
    "s_sleep 6\n"
    "buffer_load_dwordx2 v[22:23], v17, s[16:19], 0 offen offset:128 sc1\n"
    "s_waitcnt vmcnt(0)\n"
    "v_cmp_ne_u64 vcc, s[52:53], v[22:23]\n"
    "s_cbranch_vccz Lsb5_%=\n"
    "s_sub_u32 s3, s3, 1\n"
    "s_cbranch_scc0 Lsq5_%=\n"
    "s_branch Lsb5_%=\n"
    "Lsp6_%=:\n"
    "s_mov_b32 s3, 16777216\n"
    "Lsq6_%=:\n"
    "s_sleep 1\n"
    "ds_read_b32 v26, v31 offset:48\n"
    "s_waitcnt lgkmcnt(0)\n"
    "v_cmp_ne_u32 vcc, 1, v26\n"
    "s_cbranch_vccz Lsb6_%=\n"
    "s_sub_u32 s3, s3, 1\n"
    "s_cbranch_scc0 Lsq6_%=\n"
    "s_branch Lsb6_%=\n"
    "Lsp7_%=:\n"
    "s_mov_b32 s3, 4\n"
    "Lsq7_%=:\n"
    "s_sleep 1\n"
    "ds_read_b32 v26, v31 offset:16\n"
    "s_waitcnt lgkmcnt(0)\n"
    "v_cmp_ne_u32 vcc, 1, v26\n"
    "s_cbranch_vccz Lsb7_%=\n"
    "s_sub_u32 s3, s3, 1\n"
    "s_cbranch_scc0 Lsq7_%=\n"
    "s_branch Lsb7_%=\n"
    "Lsp8_%=:\n"
    "s_waitcnt vmcnt(0)\n"
    "buffer_store_dwordx2 v[18:19], v10, s[16:19], s69 offen offset:128 sc1\n"
    "v_bfrev_b32 v10, 1\n"
    "s_mov_b32 s3, 16777216\n"
    "Lsq8_%=:\n"
    "s_sleep 1\n"
    "ds_read_b32 v26, v31 offset:16\n"
    "s_waitcnt lgkmcnt(0)\n"
    "v_cmp_ne_u32 vcc, 1, v26\n"
    "s_cbranch_vccz Lsb8_%=\n"
    "s_sub_u32 s3, s3, 1\n"
    "s_cbranch_scc0 Lsq8_%=\n"
    "s_branch Lsb8_%=\n"
    "Lsp9_%=:\n"
    "s_mov_b32 s3, 262144\n"
    "Lsq9_%=:\n"
    "s_sleep 6\n"
    "buffer_load_dwordx4 v[56:59], v253, s[16:19], 0 offen offset:0 sc1\n"
    "buffer_load_dwordx4 v[60:63], v253, s[16:19], 0 offen offset:512 sc1\n"
    "s_waitcnt vmcnt(0)\n"
    "v_cmp_ne_u32 vcc, s52, v57\n"
    "v_cmp_ne_u32 s[100:101], s53, v59\n"
    "s_or_b64 vcc, vcc, s[100:101]\n"
    "s_cbranch_scc0 Lsb9_%=\n"
    "s_sub_u32 s3, s3, 1\n"
    "s_cbranch_scc0 Lsq9_%=\n"
    "s_branch Lsb9_%=\n"
    "Lsp10_%=:\n"
    "s_mov_b32 s3, 262144\n"
    "Lsq10_%=:\n"
    "s_sleep 6\n"
    "buffer_load_dwordx4 v[56:59], v253, s[16:19], 0 offen offset:0 sc1\n"
    "buffer_load_dwordx4 v[60:63], v253, s[16:19], 0 offen offset:512 sc1\n"
    "s_waitcnt vmcnt(0)\n"
    "v_cmp_ne_u32 vcc, s52, v61\n"
    "v_cmp_ne_u32 s[100:101], s53, v63\n"
    "s_or_b64 vcc, vcc, s[100:101]\n"
    "s_cbranch_scc0 Lsb10_%=\n"
    "s_sub_u32 s3, s3, 1\n"
    "s_cbranch_scc0 Lsq10_%=\n"
    "s_branch Lsb10_%=\n"
    "Lsp11_%=:\n"
    "s_waitcnt vmcnt(0)\n"
    "buffer_store_dwordx2 v[18:19], v9, s[16:19], s75 offen offset:0 sc1\n"
    "v_bfrev_b32 v255, 1\n"
    "s_mov_b32 s3, 16777216\n"
    "Lsq11_%=:\n"
    "s_sleep 2\n"
    "ds_read_b32 v26, v27 offset:32\n"
    "s_waitcnt lgkmcnt(0)\n"
    "v_cmp_ne_u32 vcc, 1, v26\n"
    "s_cbranch_vccz Lsb11_%=\n"
    "s_sub_u32 s3, s3, 1\n"
    "s_cbranch_scc0 Lsq11_%=\n"
    "s_branch Lsb11_%=\n"
    "Lsp12_%=:\n"
    "s_mov_b32 s3, 16777216\n"
    "Lsq12_%=:\n"
    "s_sleep 1\n"
    "ds_read_b32 v26, v31 offset:80\n"
    "s_waitcnt lgkmcnt(0)\n"
    "v_cmp_ne_u32 vcc, 1, v26\n"
    "s_cbranch_vccz Lsb12_%=\n"
    "s_sub_u32 s3, s3, 1\n"
    "s_cbranch_scc0 Lsq12_%=\n"
    "s_branch Lsb12_%=\n"
    "Lsp13_%=:\n"
    "s_mov_b32 s3, 262144\n"
    "Lsq13_%=:\n"
    "s_sleep 2\n"
    "buffer_load_dwordx2 v[228:229], v251, s[16:19], 0 offen sc1\n"
    "s_waitcnt vmcnt(0)\n"
    "v_cmp_ne_u64 vcc, s[52:53], v[228:229]\n"
    "s_cbranch_vccz Lsb13_%=\n"
    "s_sub_u32 s3, s3, 1\n"
    "s_cbranch_scc0 Lsq13_%=\n"
    "s_branch Lsb13_%=\n"
    "Lsp14_%=:\n"
    "s_mov_b32 s3, 262144\n"
    "Lsq14_%=:\n"
    "s_sleep 6\n"
    "buffer_load_dwordx2 v[228:229], v21, s[16:19], 0 offen sc1\n"
    "s_waitcnt vmcnt(0)\n"
    "v_cmp_ne_u64 vcc, s[52:53], v[228:229]\n"
    "s_cbranch_vccz Lsb14_%=\n"
    "s_sub_u32 s3, s3, 1\n"
    "s_cbranch_scc0 Lsq14_%=\n"
    "s_branch Lsb14_%=\n"
    "Lsp15_%=:\n"
    "s_mov_b32 s3, 16777216\n"
    "Lsq15_%=:\n"
    "s_sleep 1\n"
    "ds_read_b32 v26, v31 offset:96\n"
    "s_waitcnt lgkmcnt(0)\n"
    "v_cmp_ne_u32 vcc, 1, v26\n"
    "s_cbranch_vccz Lsb15_%=\n"
    "s_sub_u32 s3, s3, 1\n"
    "s_cbranch_scc0 Lsq15_%=\n"
    "s_branch Lsb15_%=\n"
    "Lsp16_%=:\n"
    "s_mov_b32 s3, 262144\n"
    "Lsq16_%=:\n"
    "s_sleep 8\n"
    "buffer_load_dwordx2 v[168:169], v131, s[16:19], 0 offen sc1\n"
    "s_waitcnt vmcnt(0)\n"
    "v_cmp_ne_u64 vcc, s[52:53], v[168:169]\n"
    "s_cbranch_vccz Lsb16_%=\n"
    "s_sub_u32 s3, s3, 1\n"
    "s_cbranch_scc0 Lsq16_%=\n"
    "s_branch Lsb16_%=\n"
    "Lend_%=:\n"
    :: "s"(__builtin_amdgcn_kernarg_segment_ptr()), "s"(blockIdx.x), "{v0}"(threadIdx.x), "i"((__attribute__((address_space(3))) char*)smem)
    : "v1", "v2", "v3", "v4", "v5", "v6", "v7", "v8", "v9", "v10", "v11", "v12", "v13", "v14", "v15", "v16", "v17", "v18", "v19", "v20", "v21", "v22", "v23", "v24", "v25", "v26", "v27", "v28", "v29", "v30", "v31", "v32", "v33", "v34", "v35", "v36", "v37", "v38", "v39", "v40", "v41", "v42", "v43", "v44", "v45", "v46", "v47", "v48", "v49", "v50", "v51", "v52", "v53", "v54", "v55", "v56", "v57", "v58", "v59", "v60", "v61", "v62", "v63", "v64", "v65", "v66", "v67", "v68", "v69", "v70", "v71", "v72", "v73", "v74", "v75", "v76", "v77", "v78", "v79", "v80", "v81", "v82", "v83", "v84", "v85", "v86", "v87", "v88", "v89", "v90", "v91", "v92", "v93", "v94", "v95", "v96", "v97", "v98", "v99", "v100", "v101", "v102", "v103", "v104", "v105", "v106", "v107", "v108", "v109", "v110", "v111", "v112", "v113", "v114", "v115", "v116", "v117", "v118", "v119", "v120", "v121", "v122", "v123", "v124", "v125", "v126", "v127", "v128", "v129", "v130", "v131", "v132", "v133", "v134", "v135", "v136", "v137", "v138", "v139", "v140", "v141", "v142", "v143", "v144", "v145", "v146", "v147", "v148", "v149", "v150", "v151", "v152", "v153", "v154", "v155", "v156", "v157", "v158", "v159", "v160", "v161", "v162", "v163", "v164", "v165", "v166", "v167", "v168", "v169", "v170", "v171", "v172", "v173", "v174", "v175", "v176", "v177", "v178", "v179", "v180", "v181", "v182", "v183", "v184", "v185", "v186", "v187", "v188", "v189", "v190", "v191", "v192", "v193", "v194", "v195", "v196", "v197", "v198", "v199", "v200", "v201", "v202", "v203", "v204", "v205", "v206", "v207", "v208", "v209", "v210", "v211", "v212", "v213", "v214", "v215", "v216", "v217", "v218", "v219", "v220", "v221", "v222", "v223", "v224", "v225", "v226", "v227", "v228", "v229", "v230", "v231", "v232", "v233", "v234", "v235", "v236", "v237", "v238", "v239", "v240", "v241", "v242", "v243", "v244", "v245", "v246", "v247", "v248", "v249", "v250", "v251", "v252", "v253", "v254", "v255", "s3", "s4", "s5", "s6", "s7", "s8", "s9", "s10", "s11", "s12", "s13", "s14", "s15", "s16", "s17", "s18", "s19", "s20", "s21", "s22", "s23", "s24", "s25", "s26", "s27", "s28", "s29", "s30", "s31", "s32", "s33", "s34", "s35", "s36", "s37", "s38", "s39", "s40", "s41", "s42", "s43", "s44", "s45", "s46", "s47", "s48", "s49", "s50", "s51", "s52", "s53", "s54", "s55", "s56", "s57", "s58", "s59", "s60", "s61", "s62", "s63", "s64", "s65", "s66", "s67", "s68", "s69", "s70", "s71", "s72", "s73", "s74", "s75", "s76", "s77", "s78", "s79", "s80", "s81", "s82", "s83", "s84", "s85", "s86", "s87", "s88", "s89", "s90", "s91", "s92", "s93", "s94", "s95", "s96", "s97", "s98", "s99", "s100", "s101", "vcc", "scc", "memory");
}

extern "C" void kernel_launch(void* const* d_in, const int* in_sizes, int n_in, void* d_out, int out_size, void* d_ws, size_t ws_size,
                              hipStream_t stream) {
  const unsigned f = 0x00020000u;
  fused<<<256, 512, 0, stream>>>(d_in[0], 2097152u, f, d_in[7], 1835008u, f, d_in[4], 2097152u, f, d_ws, 12582912u, f,
                                 d_in[8], 3584u, f, d_in[9], 1835008u, f, d_in[1], 2097152u, f, d_in[2], 2097152u, f,
                                 d_in[3], 4096u, f, d_in[5], 1024u, f, d_in[10], 1024u, f, d_out, 2097152u, f);
}
```
